```python
import torch

def _softmax(x, axis=-1):
    return torch.softmax(x, dim=axis)
def _gelu(x):
    return torch.nn.functional.gelu(x, approximate="tanh")
def _silu(x):
    return torch.nn.functional.silu(x)
def _sigmoid(x):
    return torch.sigmoid(x)
def _layernorm(x, axis=-1, eps=1e-5):
    m = x.mean(dim=axis, keepdim=True)
    v = x.var(dim=axis, keepdim=True, unbiased=False)
    return (x - m) / torch.sqrt(v + eps)
def _scatter_add(tgt, idx, src, axis=0):
    return tgt.clone().index_add_(axis, idx, src)
def _segment_sum(src, seg, n):
    out = torch.zeros((n,) + src.shape[1:], dtype=src.dtype)
    return out.index_add_(0, seg, src)
def _pad(x, pw):
    flat = tuple(v for pair in reversed(pw) for v in pair)
    return torch.nn.functional.pad(x, flat)

kernel_name = 'concat_heads__mha_block_cached__mha_block_cached__decode'

def setup_inputs():

    _w = (lambda a: (lambda t: t.to(torch.bfloat16) if t.is_floating_point() else t)(torch.as_tensor(a)))
    def _A_setup():
        B, Sq, D = 2, 1, 512
        H, Sk, Dh = 32, 8192, 128
        return {
            "x": _w((lambda *s: torch.randn(*s, dtype=torch.bfloat16) * 0.8)(B, Sq, D)),
            "k_cache": _w((lambda *s: torch.randn(*s, dtype=torch.bfloat16) * 0.8)(B, H, Sk, Dh)),
            "v_cache": _w((lambda *s: torch.randn(*s, dtype=torch.bfloat16) * 0.05)(B, H, Sk, Dh)),
            "wq": _w((lambda *s: torch.randn(*s, dtype=torch.bfloat16) * 0.8)(D, H * Dh) * (D ** -0.5)),
            "wo": _w((lambda *s: torch.randn(*s, dtype=torch.bfloat16) * 0.05)(H * Dh, D) * ((H * Dh) ** -0.5)),
        }
    _A_in = _A_setup()

    def _B_setup():
        B, Sq, D = 2, 1, 512
        H, Sk, Dh = 32, 8192, 128
        return {
            "x": _w((lambda *s: torch.randn(*s, dtype=torch.bfloat16) * 0.8)(B, Sq, D)),
            "k_cache": _w((lambda *s: torch.randn(*s, dtype=torch.bfloat16) * 0.8)(B, H, Sk, Dh)),
            "v_cache": _w((lambda *s: torch.randn(*s, dtype=torch.bfloat16) * 0.05)(B, H, Sk, Dh)),
            "wq": _w((lambda *s: torch.randn(*s, dtype=torch.bfloat16) * 0.8)(D, H * Dh) * (D ** -0.5)),
            "wo": _w((lambda *s: torch.randn(*s, dtype=torch.bfloat16) * 0.05)(H * Dh, D) * ((H * Dh) ** -0.5)),
        }
    _B_in = _B_setup()

    return {
        "A_x": _A_in["x"],
        "A_k_cache": _A_in["k_cache"],
        "A_v_cache": _A_in["v_cache"],
        "A_wq": _A_in["wq"],
        "A_wo": _A_in["wo"],
        "B_x": _B_in["x"],
        "B_k_cache": _B_in["k_cache"],
        "B_v_cache": _B_in["v_cache"],
        "B_wq": _B_in["wq"],
        "B_wo": _B_in["wo"],
    }

def reference(A_x, A_k_cache, A_v_cache, A_wq, A_wo, B_x, B_k_cache, B_v_cache, B_wq, B_wo):
    def _A_ref(x, k_cache, v_cache, wq, wo):
        B, Sq, D = x.shape
        H, Dh = 32, 128
        q = (lambda x, *d: x.permute(*d))((x @ wq).reshape(B, Sq, H, Dh), 0, 2, 1, 3)
        scores = torch.einsum("bhqd,bhkd->bhqk", q, k_cache) * (Dh ** -0.5)
        p = _softmax(scores, axis=-1)
        o = torch.einsum("bhqk,bhkd->bhqd", p, v_cache)
        return (lambda x, *d: x.permute(*d))(o, 0, 2, 1, 3).reshape(B, Sq, H * Dh) @ wo

    def _B_ref(x, k_cache, v_cache, wq, wo):
        B, Sq, D = x.shape
        H, Dh = 32, 128
        q = (lambda x, *d: x.permute(*d))((x @ wq).reshape(B, Sq, H, Dh), 0, 2, 1, 3)
        scores = torch.einsum("bhqd,bhkd->bhqk", q, k_cache) * (Dh ** -0.5)
        p = _softmax(scores, axis=-1)
        o = torch.einsum("bhqk,bhkd->bhqd", p, v_cache)
        return (lambda x, *d: x.permute(*d))(o, 0, 2, 1, 3).reshape(B, Sq, H * Dh) @ wo

    a = _A_ref(A_x, A_k_cache, A_v_cache, A_wq, A_wo)
    b = _B_ref(B_x, B_k_cache, B_v_cache, B_wq, B_wo)
    return torch.concatenate([a, b], axis=-1)
```

```cpp
#include <hip/hip_runtime.h>
#include <hip/hip_bf16.h>
#include <stdint.h>

typedef unsigned u32x4 __attribute__((ext_vector_type(4)));
typedef float f32x4 __attribute__((ext_vector_type(4)));
typedef __bf16 bf16x2 __attribute__((ext_vector_type(2)));

constexpr int NH = 32, SK = 8192, DH = 128, DM = 512;
constexpr int NWG = 256, NTHR = 512, NWAVE = 8;
constexpr int NSPLIT = 2;
constexpr int KEYS_WG = SK / NSPLIT;
constexpr int BLK = 32;
constexpr int NLD = BLK / 4;
constexpr int NBLK = KEYS_WG / (BLK * NWAVE);

struct Params {
  const uint16_t* x[2];
  const uint16_t* k[2];
  const uint16_t* v[2];
  const uint16_t* wq[2];
  const uint16_t* wo[2];
  uint16_t* out;
  float* ws;
};

__device__ __forceinline__ float dot2(unsigned a, unsigned b, float acc) {
  return __builtin_amdgcn_fdot2_f32_bf16(__builtin_bit_cast(bf16x2, a), __builtin_bit_cast(bf16x2, b), acc, false);
}
template <int CTRL> __device__ __forceinline__ float dpp(float x) {
  return __builtin_bit_cast(float, __builtin_amdgcn_mov_dpp(__builtin_bit_cast(int, x), CTRL, 0xf, 0xf, true));
}
constexpr int XOR1 = 0xB1, XOR2 = 0x4E, XOR7 = 0x141, XOR8 = 0x128, BCAST0 = 0x150;
__device__ __forceinline__ float bf_lo(unsigned u) { return __builtin_bit_cast(float, u << 16); }
__device__ __forceinline__ float bf_hi(unsigned u) { return __builtin_bit_cast(float, u & 0xffff0000u); }
__device__ __forceinline__ float xrow_sum(float x) {
  auto s = __builtin_amdgcn_permlane16_swap(__float_as_uint(x), __float_as_uint(x), false, false);
  x = __uint_as_float(s[0]) + __uint_as_float(s[1]);
  auto t = __builtin_amdgcn_permlane32_swap(__float_as_uint(x), __float_as_uint(x), false, false);
  return __uint_as_float(t[0]) + __uint_as_float(t[1]);
}
__device__ __forceinline__ float xrow_max(float x) {
  auto s = __builtin_amdgcn_permlane16_swap(__float_as_uint(x), __float_as_uint(x), false, false);
  x = fmaxf(__uint_as_float(s[0]), __uint_as_float(s[1]));
  auto t = __builtin_amdgcn_permlane32_swap(__float_as_uint(x), __float_as_uint(x), false, false);
  return fmaxf(__uint_as_float(t[0]), __uint_as_float(t[1]));
}
__device__ __forceinline__ u32x4 ld_nt(const void* p) { return __builtin_nontemporal_load(reinterpret_cast<const u32x4*>(p)); }
__device__ __forceinline__ u32x4 ld_pl(const void* p) { return *reinterpret_cast<const u32x4*>(p); }
__device__ __forceinline__ unsigned pack_bf16(float lo, float hi) {
  bf16x2 r;
  r[0] = (__bf16)lo;
  r[1] = (__bf16)hi;
  return __builtin_bit_cast(unsigned, r);
}
template <int T> __device__ __forceinline__ void pv_step(float (&acc)[8], float p, const u32x4& v) {
  const float pb = dpp<BCAST0 + T>(p);
#pragma unroll
  for (int k = 0; k < 4; ++k) {
    acc[2 * k] = fmaf(pb, bf_lo(v[k]), acc[2 * k]);
    acc[2 * k + 1] = fmaf(pb, bf_hi(v[k]), acc[2 * k + 1]);
  }
}

constexpr int WS_ML = 0, WS_Y = 1024;

__global__ void __launch_bounds__(NTHR, 2) attn_kernel(Params P) {
  __shared__ __attribute__((aligned(16))) float lds[NWAVE * 136 + 256];
  const int bid = blockIdx.x;
  const int xg = bid & 7, j = bid >> 3;
  const int ph = xg * 8 + (j >> 2), sub = j & 3;
  const int prob = ph >> 5, h = ph & 31, b = sub >> 1, split = sub & 1;
  const int tid = threadIdx.x, lane = tid & 63;
  const int wave = __builtin_amdgcn_readfirstlane(tid >> 6);
  const int g = lane >> 4, c = lane & 15;

  const uint16_t* xp = P.x[prob] + b * DM;
  const uint16_t* wqp = P.wq[prob] + (size_t)(64 * wave + g) * (NH * DH) + h * DH + 8 * c;
  const size_t kvoff = ((size_t)(b * NH + h) * SK + split * KEYS_WG + wave * BLK + g) * DH + 8 * c;
  const uint16_t* kp = P.k[prob] + kvoff;
  const uint16_t* vp = P.v[prob] + kvoff;

  const float xv = bf_lo((unsigned)xp[64 * wave + 4 * c + g]);
  u32x4 W[16];
#pragma unroll
  for (int t = 0; t < 16; ++t) W[t] = ld_pl(wqp + (size_t)t * 4 * (NH * DH));
  u32x4 K0[NLD], V0[NLD], K1[NLD], V1[NLD];
  constexpr size_t BSTR = (size_t)BLK * NWAVE * DH;
#pragma unroll
  for (int t = 0; t < NLD; ++t) K0[t] = ld_nt(kp + t * 4 * DH);
#pragma unroll
  for (int t = 0; t < NLD; ++t) V0[t] = ld_nt(vp + t * 4 * DH);
#pragma unroll
  for (int t = 0; t < NLD; ++t) K1[t] = ld_nt(kp + BSTR + t * 4 * DH);
#pragma unroll
  for (int t = 0; t < NLD; ++t) V1[t] = ld_nt(vp + BSTR + t * 4 * DH);

  unsigned q[4];
  {
    float a[8];
#pragma unroll
    for (int k = 0; k < 8; ++k) a[k] = 0.f;
    pv_step<0>(a, xv, W[0]); pv_step<1>(a, xv, W[1]); pv_step<2>(a, xv, W[2]); pv_step<3>(a, xv, W[3]);
    pv_step<4>(a, xv, W[4]); pv_step<5>(a, xv, W[5]); pv_step<6>(a, xv, W[6]); pv_step<7>(a, xv, W[7]);
    pv_step<8>(a, xv, W[8]); pv_step<9>(a, xv, W[9]); pv_step<10>(a, xv, W[10]); pv_step<11>(a, xv, W[11]);
    pv_step<12>(a, xv, W[12]); pv_step<13>(a, xv, W[13]); pv_step<14>(a, xv, W[14]); pv_step<15>(a, xv, W[15]);
#pragma unroll
    for (int k = 0; k < 8; ++k) a[k] = xrow_sum(a[k]);
    if (g == 0) {
      *reinterpret_cast<f32x4*>(&lds[wave * 128 + 8 * c]) = f32x4{a[0], a[1], a[2], a[3]};
      *reinterpret_cast<f32x4*>(&lds[wave * 128 + 8 * c + 4]) = f32x4{a[4], a[5], a[6], a[7]};
    }
    __builtin_amdgcn_s_waitcnt(0xC07F);
    __builtin_amdgcn_s_barrier();
    f32x4 s0 = {0.f, 0.f, 0.f, 0.f}, s1 = {0.f, 0.f, 0.f, 0.f};
#pragma unroll
    for (int w = 0; w < NWAVE; ++w) {
      s0 += *reinterpret_cast<const f32x4*>(&lds[w * 128 + 8 * c]);
      s1 += *reinterpret_cast<const f32x4*>(&lds[w * 128 + 8 * c + 4]);
    }
    q[0] = pack_bf16(s0[0], s0[1]); q[1] = pack_bf16(s0[2], s0[3]);
    q[2] = pack_bf16(s1[0], s1[1]); q[3] = pack_bf16(s1[2], s1[3]);
  }

  constexpr float C2 = 0.08838834764831845f * 1.4426950408889634f;
  float m = -INFINITY, l = 0.f, acc[8];
#pragma unroll
  for (int k = 0; k < 8; ++k) acc[k] = 0.f;

  auto scores = [&](const u32x4 (&K)[NLD]) -> float {
    float x[NLD];
#pragma unroll
    for (int t = 0; t < NLD; ++t) {
      float d = dot2(K[t][0], q[0], 0.f);
      d = dot2(K[t][1], q[1], d);
      d = dot2(K[t][2], q[2], d);
      x[t] = dot2(K[t][3], q[3], d);
    }
#pragma unroll
    for (int i = 0; i < 4; ++i) { const float t1 = x[i] + dpp<XOR8>(x[i]), t2 = x[i + 4] + dpp<XOR8>(x[i + 4]); x[i] = (c & 8) ? t2 : t1; }
#pragma unroll
    for (int i = 0; i < 2; ++i) { const float t1 = x[i] + dpp<XOR7>(x[i]), t2 = x[i + 2] + dpp<XOR7>(x[i + 2]); x[i] = (c & 4) ? t2 : t1; }
    { const float t1 = x[0] + dpp<XOR2>(x[0]), t2 = x[1] + dpp<XOR2>(x[1]); x[0] = (c & 2) ? t2 : t1; }
    const float s = (x[0] + dpp<XOR1>(x[0])) * C2;
    float pm = fmaxf(s, dpp<XOR8>(s));
    pm = fmaxf(pm, dpp<XOR7>(pm));
    pm = fmaxf(pm, dpp<XOR2>(pm));
    const float mn = fmaxf(m, pm);
    const float alpha = __builtin_amdgcn_exp2f(m - mn);
    const float p = __builtin_amdgcn_exp2f(s - mn);
    m = mn;
    l = fmaf(l, alpha, p);
#pragma unroll
    for (int k = 0; k < 8; ++k) acc[k] *= alpha;
    return p;
  };
  auto pv = [&](float p, const u32x4 (&V)[NLD]) {
    pv_step<0>(acc, p, V[0]); pv_step<2>(acc, p, V[1]); pv_step<4>(acc, p, V[2]); pv_step<6>(acc, p, V[3]);
    pv_step<8>(acc, p, V[4]); pv_step<10>(acc, p, V[5]); pv_step<12>(acc, p, V[6]); pv_step<14>(acc, p, V[7]);
  };

#pragma unroll 1
  for (int i = 0; i < NBLK - 2; i += 2) {
    const uint16_t* kn = kp + (size_t)(i + 2) * BSTR;
    const uint16_t* vn = vp + (size_t)(i + 2) * BSTR;
    float p = scores(K0);
#pragma unroll
    for (int t = 0; t < NLD; ++t) K0[t] = ld_nt(kn + t * 4 * DH);
    pv(p, V0);
#pragma unroll
    for (int t = 0; t < NLD; ++t) V0[t] = ld_nt(vn + t * 4 * DH);
    p = scores(K1);
#pragma unroll
    for (int t = 0; t < NLD; ++t) K1[t] = ld_nt(kn + BSTR + t * 4 * DH);
    pv(p, V1);
#pragma unroll
    for (int t = 0; t < NLD; ++t) V1[t] = ld_nt(vn + BSTR + t * 4 * DH);
  }
  const uint16_t* wop = P.wo[prob] + (size_t)(h * DH + (lane >> 3)) * DM + 64 * wave + 8 * (lane & 7);
  {
    float p = scores(K0);
#pragma unroll
    for (int t = 0; t < 8; ++t) W[t] = ld_pl(wop + (size_t)t * 8 * DM);
    pv(p, V0);
#pragma unroll
    for (int t = 8; t < 16; ++t) W[t] = ld_pl(wop + (size_t)t * 8 * DM);
    p = scores(K1);
    pv(p, V1);
  }
  const float mw = xrow_max(m);
  const float sc = __builtin_amdgcn_exp2f(m - mw);
  l *= sc;
  l += dpp<XOR8>(l); l += dpp<XOR7>(l); l += dpp<XOR2>(l);
  l = xrow_sum(l);
#pragma unroll
  for (int k = 0; k < 8; ++k) acc[k] = xrow_sum(acc[k] * sc);
  __builtin_amdgcn_s_barrier();
  if (g == 0) {
    *reinterpret_cast<f32x4*>(&lds[wave * 136 + 8 * c]) = f32x4{acc[0], acc[1], acc[2], acc[3]};
    *reinterpret_cast<f32x4*>(&lds[wave * 136 + 8 * c + 4]) = f32x4{acc[4], acc[5], acc[6], acc[7]};
    if (c == 0) { lds[wave * 136 + 128] = mw; lds[wave * 136 + 129] = l; }
  }
  __builtin_amdgcn_s_waitcnt(0xC07F);
  __builtin_amdgcn_s_barrier();
  float* ov = &lds[NWAVE * 136];
  float M = -INFINITY;
#pragma unroll
  for (int w = 0; w < NWAVE; ++w) M = fmaxf(M, lds[w * 136 + 128]);
  if (tid < 128) {
    float o = 0.f, L = 0.f;
#pragma unroll
    for (int w = 0; w < NWAVE; ++w) {
      const float wt = __builtin_amdgcn_exp2f(lds[w * 136 + 128] - M);
      o = fmaf(wt, lds[w * 136 + tid], o);
      L = fmaf(wt, lds[w * 136 + 129], L);
    }
    ov[tid] = o;
    if (tid == 0) { P.ws[WS_ML + 2 * bid] = M; P.ws[WS_ML + 2 * bid + 1] = L; }
  }
  __builtin_amdgcn_s_waitcnt(0xC07F);
  __builtin_amdgcn_s_barrier();
  float y[8];
#pragma unroll
  for (int k = 0; k < 8; ++k) y[k] = 0.f;
#pragma unroll
  for (int t = 0; t < 16; ++t) {
    const float o = ov[(lane >> 3) + 8 * t];
#pragma unroll
    for (int k = 0; k < 4; ++k) {
      y[2 * k] = fmaf(o, bf_lo(W[t][k]), y[2 * k]);
      y[2 * k + 1] = fmaf(o, bf_hi(W[t][k]), y[2 * k + 1]);
    }
  }
#pragma unroll
  for (int k = 0; k < 8; ++k) { y[k] += dpp<XOR8>(y[k]); y[k] = xrow_sum(y[k]); }
  if (lane < 8) {
    float* yp = P.ws + WS_Y + (size_t)bid * DM + 64 * wave + 8 * lane;
    *reinterpret_cast<f32x4*>(yp) = f32x4{y[0], y[1], y[2], y[3]};
    *reinterpret_cast<f32x4*>(yp + 4) = f32x4{y[4], y[5], y[6], y[7]};
  }
}

__global__ void __launch_bounds__(256) combine_kernel(const float* ws, uint16_t* out) {
  __shared__ float red[256];
  const int blk = blockIdx.x, pb = blk >> 3, cg = blk & 7, prob = pb >> 1, b = pb & 1;
  const int tid = threadIdx.x, part = tid >> 6, col = cg * 64 + (tid & 63);
  float sum = 0.f;
#pragma unroll
  for (int i = 0; i < 8; ++i) {
    const int h = part * 8 + i, ph = prob * 32 + h;
    const int xg = ph >> 3, j0 = (ph & 7) * 4 + b * 2;
    const int b0 = xg + 8 * j0, b1 = b0 + 8;
    const float m0 = ws[WS_ML + 2 * b0], l0 = ws[WS_ML + 2 * b0 + 1];
    const float m1 = ws[WS_ML + 2 * b1], l1 = ws[WS_ML + 2 * b1 + 1];
    const float M = fmaxf(m0, m1);
    const float w0 = __builtin_amdgcn_exp2f(m0 - M), w1 = __builtin_amdgcn_exp2f(m1 - M);
    const float inv = 1.f / (w0 * l0 + w1 * l1);
    sum += (w0 * ws[WS_Y + (size_t)b0 * DM + col] + w1 * ws[WS_Y + (size_t)b1 * DM + col]) * inv;
  }
  red[tid] = sum;
  __syncthreads();
  if (tid < 64) {
    const float r = (red[tid] + red[tid + 64]) + (red[tid + 128] + red[tid + 192]);
    const __bf16 o = (__bf16)r;
    out[b * (2 * DM) + prob * DM + col] = __builtin_bit_cast(uint16_t, o);
  }
}

extern "C" void kernel_launch(void* const* d_in, const int* in_sizes, int n_in, void* d_out, int out_size, void* d_ws,
                              size_t ws_size, hipStream_t stream) {
  Params P;
  for (int p = 0; p < 2; ++p) {
    P.x[p] = (const uint16_t*)d_in[5 * p + 0];
    P.k[p] = (const uint16_t*)d_in[5 * p + 1];
    P.v[p] = (const uint16_t*)d_in[5 * p + 2];
    P.wq[p] = (const uint16_t*)d_in[5 * p + 3];
    P.wo[p] = (const uint16_t*)d_in[5 * p + 4];
  }
  P.out = (uint16_t*)d_out;
  P.ws = (float*)d_ws;
  attn_kernel<<<NWG, NTHR, 0, stream>>>(P);
  combine_kernel<<<32, 256, 0, stream>>>((const float*)d_ws, (uint16_t*)d_out);
}
```

```cpp
#include <hip/hip_runtime.h>
#include <hip/hip_bf16.h>
#include <stdint.h>

typedef unsigned u32x4 __attribute__((ext_vector_type(4)));
typedef unsigned u32x2 __attribute__((ext_vector_type(2)));
typedef float f32x4 __attribute__((ext_vector_type(4)));
typedef float f32x2 __attribute__((ext_vector_type(2)));
typedef __bf16 bf16x2 __attribute__((ext_vector_type(2)));
typedef __bf16 bf16x8 __attribute__((ext_vector_type(8)));
#define LAS __attribute__((address_space(3)))
#define GAS __attribute__((address_space(1)))

#define PACE_DQ 14420
#define PACE_T0 130
#define ROT_BYTES 16384
#define Q_DELAY 60
#define Q_RETRY 0
#define EARLY 1
#define WO_GAP 56

constexpr int NH = 32, SK = 8192, DH = 128, DM = 512;
constexpr int NWG = 256, NTHR = 512;
constexpr unsigned REGION = 1u << 20;
constexpr int NT = 2;
#define X_AUX 2
#define HL_AUX 16
#define HS_AUX 16
constexpr float C2 = 0.08838834764831845f * 1.4426950408889634f;

constexpr unsigned WS_EPOCH = 0, WS_ML = 4096, WS_QX = 8192, WS_Y = WS_QX + 64 * 8192, WS_END = WS_Y + NWG * 4096;
constexpr int L_SC = 0;
constexpr int L_OP = L_SC + 4096;
constexpr int L_Q = L_OP + 512;
constexpr int L_ML = L_Q + 64;
constexpr int L_TAG = L_ML + 16;
constexpr int L_END = L_TAG + 4;

struct Params {
  const unsigned char* x[2];
  const unsigned char* k[2];
  const unsigned char* v[2];
  const unsigned char* wq[2];
  const unsigned char* wo[2];
  unsigned char* out;
  unsigned char* ws;
};

__device__ __forceinline__ float dot2(unsigned a, unsigned b, float acc) {
  return __builtin_amdgcn_fdot2_f32_bf16(__builtin_bit_cast(bf16x2, a), __builtin_bit_cast(bf16x2, b), acc, false);
}
template <int CTRL> __device__ __forceinline__ unsigned dppu(unsigned x) {
  return (unsigned)__builtin_amdgcn_mov_dpp((int)x, CTRL, 0xf, 0xf, true);
}
template <int CTRL> __device__ __forceinline__ float dpp(float x) { return __builtin_bit_cast(float, dppu<CTRL>(__builtin_bit_cast(unsigned, x))); }
constexpr int XOR1 = 0xB1, XOR2 = 0x4E, XOR7 = 0x141, XOR8 = 0x128, ROR4 = 0x124, BCAST0 = 0x150;
__device__ __forceinline__ float bf_lo(unsigned u) { return __builtin_bit_cast(float, u << 16); }
__device__ __forceinline__ float bf_hi(unsigned u) { return __builtin_bit_cast(float, u & 0xffff0000u); }
__device__ __forceinline__ float swap16_add(float a, float b) {
  auto s = __builtin_amdgcn_permlane16_swap(__float_as_uint(a), __float_as_uint(b), false, false);
  return __uint_as_float(s[0]) + __uint_as_float(s[1]);
}
__device__ __forceinline__ float swap32_add(float a, float b) {
  auto s = __builtin_amdgcn_permlane32_swap(__float_as_uint(a), __float_as_uint(b), false, false);
  return __uint_as_float(s[0]) + __uint_as_float(s[1]);
}
__device__ __forceinline__ float xrow_max(float x) {
  auto s = __builtin_amdgcn_permlane16_swap(__float_as_uint(x), __float_as_uint(x), false, false);
  x = fmaxf(__uint_as_float(s[0]), __uint_as_float(s[1]));
  auto t = __builtin_amdgcn_permlane32_swap(__float_as_uint(x), __float_as_uint(x), false, false);
  return fmaxf(__uint_as_float(t[0]), __uint_as_float(t[1]));
}
__device__ __forceinline__ f32x2 fold_rows(const float (&a)[8]) {
  const float z0 = swap32_add(a[0], a[4]), z1 = swap32_add(a[1], a[5]), z2 = swap32_add(a[2], a[6]), z3 = swap32_add(a[3], a[7]);
  return f32x2{swap16_add(z0, z2), swap16_add(z1, z3)};
}
__device__ __forceinline__ unsigned pack_bf16(float lo, float hi) {
  bf16x2 r;
  r[0] = (__bf16)lo;
  r[1] = (__bf16)hi;
  return __builtin_bit_cast(unsigned, r);
}
template <int T> __device__ __forceinline__ void axpy8(float (&acc)[8], float p, const u32x4& v) {
  const float pb = dpp<BCAST0 + T>(p);
#pragma unroll
  for (int k = 0; k < 4; ++k) {
    acc[2 * k] = fmaf(pb, bf_lo(v[k]), acc[2 * k]);
    acc[2 * k + 1] = fmaf(pb, bf_hi(v[k]), acc[2 * k + 1]);
  }
}
__device__ __forceinline__ float round24(float v) { return __uint_as_float((__float_as_uint(v) + 0x80u) & 0xffffff00u); }
__device__ __forceinline__ u32x4 granules(float v0, float v1, unsigned tlo, unsigned thi) {
  return u32x4{tlo, __float_as_uint(round24(v0)) | thi, tlo, __float_as_uint(round24(v1)) | thi};
}
__device__ __forceinline__ bool valid(const u32x4& e, unsigned tlo, unsigned thi) {
  return e[0] == tlo && e[2] == tlo && (e[1] & 0xffu) == thi && (e[3] & 0xffu) == thi;
}
__device__ __forceinline__ float val(unsigned u) { return __uint_as_float(u & 0xffffff00u); }
template <int N> __device__ __forceinline__ void wait_vm() { __builtin_amdgcn_s_waitcnt((N & 15) | ((N >> 4) << 14) | (7 << 4) | (15 << 8)); }
__device__ __forceinline__ void wait_lgkm0() { __builtin_amdgcn_s_waitcnt(0xC07F); }
__device__ __forceinline__ unsigned now() { return (unsigned)__builtin_amdgcn_s_memrealtime(); }
#define PIN() __builtin_amdgcn_sched_barrier(0)
constexpr unsigned SPIN_LIMIT = 5000000u;

#define STAMP(i) do { } while (0)
#define STAMP_FLUSH() do { } while (0)

__global__ void __launch_bounds__(NTHR, 2) mha_decode_kernel(Params P) {
  __shared__ __attribute__((aligned(16))) float lds_raw[L_END];
  LAS float* lds = (LAS float*)lds_raw;
  STAMP(0);
  const unsigned t0 = now();
  const int bid = blockIdx.x;
  const int ph = (bid & 7) * 8 + (bid >> 5), sub = (bid >> 3) & 3;
  const int prob = ph >> 5, h = ph & 31, b = sub >> 1, split = sub & 1;
  const int lane = threadIdx.x & 63;
  const int wave = __builtin_amdgcn_readfirstlane(threadIdx.x >> 6);
  const int g = lane >> 4, c = lane & 15;
  const unsigned mem = 2 * h + split;
  const unsigned grp = 2 * prob + b;

  const unsigned char *px = prob ? P.x[1] : P.x[0], *pk = prob ? P.k[1] : P.k[0], *pv = prob ? P.v[1] : P.v[0];
  const unsigned char *pwq = prob ? P.wq[1] : P.wq[0], *pwo = prob ? P.wo[1] : P.wo[0];
  unsigned char *pout = P.out, *pws = P.ws;
  asm volatile("" : "+s"(px), "+s"(pk), "+s"(pv), "+s"(pwq), "+s"(pwo), "+s"(pout), "+s"(pws));

  if (wave < 4) {
    __builtin_amdgcn_s_setprio(3);
    const unsigned region = b * 64 + mem;
    const auto rk = __builtin_amdgcn_make_buffer_rsrc((void*)(pk + (size_t)region * REGION), 0, (int)REGION, 0x00020000);
    const auto rv = __builtin_amdgcn_make_buffer_rsrc((void*)(pv + (size_t)region * REGION), 0, (int)REGION, 0x00020000);
    const unsigned voff = wave * 4096 + lane * 16;
    unsigned so = region * ROT_BYTES;
    unsigned tq = 0;
    u32x4 R[8][4];
    auto issue = [&](u32x4 (&dst)[4], const __amdgpu_buffer_rsrc_t& r) {
      const unsigned s = so & (REGION - 1);
#pragma unroll
      for (int t = 0; t < 4; ++t) dst[t] = __builtin_amdgcn_raw_buffer_load_b128(r, voff + t * 1024, s, NT);
      so += 16384;
      tq += PACE_DQ;
      PIN();
    };
    auto gate = [&]() {
      PIN();
      wait_vm<4>();
      while ((int)((now() << 8) - tq) < 0) __builtin_amdgcn_s_sleep(1);
      PIN();
    };
    tq = (t0 + PACE_T0) << 8;
#pragma unroll
    for (int j = 0; j < 8; ++j) { if (j >= EARLY) gate(); if (j == 0) STAMP(6); if (j == 2) STAMP(7); if (j == 3) STAMP(8); if (j == 5) STAMP(9); issue(R[j], rk); }
    STAMP(1);
    __builtin_amdgcn_s_barrier();
    PIN();
    const u32x4 q = *(LAS const u32x4*)(lds + L_Q + 4 * c);
    STAMP(2);
    LAS float* sc = lds + L_SC + wave * 1024 + lane;
    float keep = 0.f, mx = -INFINITY;
    auto score = [&](const u32x4 (&K)[4], int J) {
      float x[4];
#pragma unroll
      for (int t = 0; t < 4; ++t) {
        float d = dot2(K[t][0], q[0], 0.f);
        d = dot2(K[t][1], q[1], d);
        d = dot2(K[t][2], q[2], d);
        x[t] = dot2(K[t][3], q[3], d);
      }
      const float a0 = x[0] + dpp<XOR8>(x[0]), a1 = x[1] + dpp<XOR8>(x[1]), a2 = x[2] + dpp<XOR8>(x[2]), a3 = x[3] + dpp<XOR8>(x[3]);
      const float y0 = (c & 8) ? a2 : a0, y1 = (c & 8) ? a3 : a1;
      const float b0 = y0 + dpp<XOR7>(y0), b1 = y1 + dpp<XOR7>(y1);
      float s = (c & 4) ? b1 : b0;
      s += dpp<XOR2>(s);
      s += dpp<XOR1>(s);
      keep = ((c & 3) == J) ? s : keep;
      asm volatile("" : "+v"(keep));
    };
#pragma unroll 1
    for (int it = 0; it < 7; ++it) {
#pragma unroll
      for (int j = 0; j < 8; ++j) {
        score(R[j], j & 3);
        if ((j & 3) == 3) { sc[(2 * it + (j >> 2)) * 64] = keep; mx = fmaxf(mx, keep); }
        gate();
        issue(R[j], rk);
      }
    }
#pragma unroll
    for (int j = 0; j < 6; ++j) {
      score(R[j], j & 3);
      if (j == 3) { sc[14 * 64] = keep; mx = fmaxf(mx, keep); }
    }
    gate();
    issue(R[0], rv);
    score(R[6], 2);
    gate();
    issue(R[1], rv);
    score(R[7], 3);
    sc[15 * 64] = keep;
    mx = fmaxf(mx, keep);
    mx = fmaxf(mx, dpp<XOR1>(mx)); mx = fmaxf(mx, dpp<XOR2>(mx)); mx = fmaxf(mx, dpp<XOR7>(mx)); mx = fmaxf(mx, dpp<XOR8>(mx));
    mx = xrow_max(mx);
    const float m2 = mx * C2;
    {
      float l = 0.f;
#pragma unroll
      for (int u = 0; u < 16; ++u) {
        const float p = __builtin_amdgcn_exp2f(fmaf(sc[u * 64], C2, -m2));
        sc[u * 64] = p;
        l += p;
      }
      l += dpp<XOR1>(l); l += dpp<XOR2>(l); l += dpp<XOR7>(l); l += dpp<XOR8>(l);
      l = swap16_add(l, l);
      l = swap32_add(l, l);
      if (lane == 0) {
        *(LAS f32x2*)(lds + L_ML + 4 * wave) = f32x2{m2, l};
        wait_lgkm0();
        *(volatile LAS unsigned*)(lds + L_ML + 4 * wave + 2) = 1u;
      }
    }
    STAMP(3);
    float acc[8];
#pragma unroll
    for (int k = 0; k < 8; ++k) acc[k] = 0.f;
#define PV(RR, J) do { axpy8<0 + J>(acc, p, RR[0]); axpy8<4 + J>(acc, p, RR[1]); axpy8<8 + J>(acc, p, RR[2]); axpy8<12 + J>(acc, p, RR[3]); \
    asm volatile("" : "+v"(acc[0]), "+v"(acc[1]), "+v"(acc[2]), "+v"(acc[3]), "+v"(acc[4]), "+v"(acc[5]), "+v"(acc[6]), "+v"(acc[7])); } while (0)
    float p = sc[0];
#pragma unroll 1
    for (int u = 0; u < 15; ++u) {
      const float pn = sc[(u + 1) * 64];
      gate(); issue(R[2], rv); PV(R[0], 0);
      gate(); issue(R[3], rv); PV(R[1], 1);
      gate(); issue(R[0], rv); PV(R[2], 2);
      gate(); issue(R[1], rv); PV(R[3], 3);
      p = pn;
    }
    gate(); issue(R[2], rv); PV(R[0], 0);
    gate(); issue(R[3], rv); PV(R[1], 1);
    STAMP(4);
    PIN();
    PV(R[2], 2);
    PV(R[3], 3);
#undef PV
    *(LAS f32x2*)(lds + L_OP + wave * 128 + 2 * lane) = fold_rows(acc);
    wait_lgkm0();
    STAMP(5);
    __builtin_amdgcn_s_barrier();
    STAMP_FLUSH();
    return;
  }

  const int hw = wave - 4;
  u32x4 W[32];
  GAS unsigned long long* epw = (GAS unsigned long long*)(pws + WS_EPOCH + prob * 256);
  const auto rw = __builtin_amdgcn_make_buffer_rsrc((void*)pws, 0, (int)WS_END, 0x00020000);
  bool ok = true;
  if (hw == 0) {
    const auto rq = __builtin_amdgcn_make_buffer_rsrc((void*)(pwq + (size_t)sub * 128 * (NH * DH * 2) + h * (DH * 2)), 0, 128 * NH * DH * 2 - h * (DH * 2), 0x00020000);
    const auto rx = __builtin_amdgcn_make_buffer_rsrc((void*)px, 0, 2 * DM * 2, 0x00020000);
    const unsigned xo = (128 * sub + 4 * c + g) * 2;
    const unsigned x00 = __builtin_amdgcn_raw_buffer_load_b16(rx, xo, 0, X_AUX), x01 = __builtin_amdgcn_raw_buffer_load_b16(rx, xo + DM * 2, 0, X_AUX);
    const unsigned x10 = __builtin_amdgcn_raw_buffer_load_b16(rx, xo + 128, 0, X_AUX), x11 = __builtin_amdgcn_raw_buffer_load_b16(rx, xo + 128 + DM * 2, 0, X_AUX);
    PIN();
    const unsigned vo = g * (NH * DH * 2) + c * 16;
#pragma unroll
    for (int t = 0; t < 32; ++t) W[t] = __builtin_amdgcn_raw_buffer_load_b128(rq, vo, t * 4 * (NH * DH * 2), NT);
    const unsigned long long prev = __hip_atomic_load(epw, __ATOMIC_RELAXED, __HIP_MEMORY_SCOPE_AGENT);
    PIN();
    STAMP(7);
    if (lane < 4) *(volatile LAS unsigned*)(lds + L_ML + 4 * lane + 2) = 0u;
    const unsigned xa = x00 | (x01 << 16), xb = x10 | (x11 << 16);
    unsigned sel[4];
#pragma unroll
    for (int k = 0; k < 4; ++k) {
      const unsigned pick = (c & 8) ? 0x0302u : 0x0100u;
      sel[k] = ((c & 7) == 2 * k ? pick : 0x0c0cu) | (((c & 7) == 2 * k + 1 ? pick : 0x0c0cu) << 16);
    }
    f32x4 d0 = {0.f, 0.f, 0.f, 0.f}, d1 = {0.f, 0.f, 0.f, 0.f};
#define QM(T, X, D) { const unsigned xt = dppu<BCAST0 + ((T) & 15)>(X); \
      const u32x4 a = {__builtin_amdgcn_perm(xt, xt, sel[0]), __builtin_amdgcn_perm(xt, xt, sel[1]), __builtin_amdgcn_perm(xt, xt, sel[2]), __builtin_amdgcn_perm(xt, xt, sel[3])}; \
      D = __builtin_amdgcn_mfma_f32_16x16x32_bf16(__builtin_bit_cast(bf16x8, a), __builtin_bit_cast(bf16x8, W[T]), D, 0, 0, 0); }
#define QM4(T, X) QM(T, X, d0) QM(T + 1, X, d1) QM(T + 2, X, d0) QM(T + 3, X, d1) PIN();
    QM4(0, xa) QM4(4, xa) QM4(8, xa) QM4(12, xa) QM4(16, xb) QM4(20, xb) QM4(24, xb) QM4(28, xb)
#undef QM4
#undef QM
    d0 += d1;
    const unsigned long long tg = prev + 1ull;
    const unsigned tlo = __builtin_amdgcn_readfirstlane((unsigned)tg), thi = __builtin_amdgcn_readfirstlane((unsigned)(tg >> 32)) & 0xffu;
    STAMP(8);
    const unsigned qo = WS_QX + ph * 8192 + sub * 2048 + (g >> 1) * 1024 + c * 64 + (g & 1) * 32;
    __builtin_amdgcn_raw_buffer_store_b128(granules(d0[0], d0[1], tlo, thi), rw, qo, 0, HS_AUX);
    __builtin_amdgcn_raw_buffer_store_b128(granules(d0[2], d0[3], tlo, thi), rw, qo + 16, 0, HS_AUX);
    STAMP(9);
    const unsigned ts = now();
    unsigned tp = ts + Q_DELAY;
    u32x4 e[4];
    do {
      while ((int)(now() - tp) < 0) __builtin_amdgcn_s_sleep(1);
      tp = now() + Q_RETRY;
#pragma unroll
      for (int k = 0; k < 4; ++k) e[k] = __builtin_amdgcn_raw_buffer_load_b128(rw, WS_QX + ph * 8192 + b * 1024 + lane * 16, k * 2048, HL_AUX);
      ok = __all(valid(e[0], tlo, thi) && valid(e[1], tlo, thi) && valid(e[2], tlo, thi) && valid(e[3], tlo, thi));
    } while (!ok && now() - ts <= SPIN_LIMIT);
    STAMP(10);
    *(LAS unsigned*)(lds + L_Q + lane) = pack_bf16((val(e[0][1]) + val(e[1][1])) + (val(e[2][1]) + val(e[3][1])), (val(e[0][3]) + val(e[1][3])) + (val(e[2][3]) + val(e[3][3])));
    if (lane == 0) *(LAS u32x2*)(lds + L_TAG) = u32x2{tlo, thi};
    wait_lgkm0();
  }
  STAMP(1);
  __builtin_amdgcn_s_barrier();
  PIN();
  const u32x2 tg2 = *(LAS const u32x2*)(lds + L_TAG);
  const unsigned tlo = __builtin_amdgcn_readfirstlane(tg2[0]), thi = __builtin_amdgcn_readfirstlane(tg2[1]);
  const unsigned yoff = WS_Y + (grp * 64 + mem) * 4096 + hw * 1024 + c * 64 + g * 16;
  {
    const auto ro = __builtin_amdgcn_make_buffer_rsrc((void*)pwo, 0, DM * NH * DH * 2, 0x00020000);
    const unsigned vo = (unsigned)(h * DH + 2 * g) * (DM * 2) + hw * 256 + c * 16;
    unsigned tw = now();
#pragma unroll
    for (int t = 0; t < 32; ++t) {
      PIN();
      while ((int)(now() - tw) < 0) __builtin_amdgcn_s_sleep(2);
      tw += WO_GAP;
      W[t] = __builtin_amdgcn_raw_buffer_load_b128(ro, vo, ((t >> 1) * 8 + (t & 1)) * (DM * 2), NT);
      PIN();
    }
#pragma unroll
    for (int u = 0; u < 16; ++u) {
      const u32x4 r0 = W[2 * u], r1 = W[2 * u + 1];
#pragma unroll
      for (int k = 0; k < 2; ++k) {
        W[2 * u][2 * k] = __builtin_amdgcn_perm(r1[k], r0[k], 0x05040100u);
        W[2 * u][2 * k + 1] = __builtin_amdgcn_perm(r1[k], r0[k], 0x07060302u);
        W[2 * u + 1][2 * k] = __builtin_amdgcn_perm(r1[k + 2], r0[k + 2], 0x05040100u);
        W[2 * u + 1][2 * k + 1] = __builtin_amdgcn_perm(r1[k + 2], r0[k + 2], 0x07060302u);
      }
    }
#pragma unroll
    for (int t = 0; t < 32; ++t) asm volatile("" : "+v"(W[t]));
  }
  STAMP(2);
  float f[4];
  float wgt[4] = {0.f, 0.f, 0.f, 0.f};
  {
    const unsigned ts = now();
    for (;;) {
      const unsigned fl = *(volatile LAS unsigned*)(lds + L_ML + 4 * (lane & 3) + 2);
      if (__all(fl == 1u) || now() - ts > SPIN_LIMIT) break;
      __builtin_amdgcn_s_sleep(32);
    }
    asm volatile("" ::: "memory");
    float mw[4], lw[4];
#pragma unroll
    for (int w = 0; w < 4; ++w) { const f32x2 ml = *(volatile LAS f32x2*)(lds + L_ML + 4 * w); mw[w] = ml[0]; lw[w] = ml[1]; }
    const float m = round24(fmaxf(fmaxf(mw[0], mw[1]), fmaxf(mw[2], mw[3])));
    float l = 0.f;
#pragma unroll
    for (int w = 0; w < 4; ++w) { f[w] = __builtin_amdgcn_exp2f(mw[w] - m); l = fmaf(f[w], lw[w], l); }
    if (hw == 0) {
      if (lane == 0)
        __builtin_amdgcn_raw_buffer_store_b128(granules(m, l, tlo, thi), rw, WS_ML + (grp * 64 + mem) * 16, 0, HS_AUX);
      u32x4 e;
      for (;;) {
        __builtin_amdgcn_s_sleep(100);
        e = __builtin_amdgcn_raw_buffer_load_b128(rw, WS_ML + (grp * 64 + lane) * 16, 0, HL_AUX);
        if (__all(valid(e, tlo, thi))) break;
        if (now() - ts > SPIN_LIMIT) { ok = false; break; }
      }
      const float m0 = val(e[1]), l0 = val(e[3]);
      const float m1 = dpp<XOR1>(m0), l1 = dpp<XOR1>(l0);
      const float M = fmaxf(m0, m1);
      const float e0 = __builtin_amdgcn_exp2f(m0 - M), e1 = __builtin_amdgcn_exp2f(m1 - M);
      const float wv = e0 / fmaf(e0, l0, e1 * l1);
#pragma unroll
      for (int k = 0; k < 4; ++k) wgt[k] = __shfl(wv, 16 * k + (lane >> 2));
    }
  }
  STAMP(3);
  __builtin_amdgcn_s_setprio(3);
  __builtin_amdgcn_s_barrier();
  PIN();
  STAMP(4);
  {
    f32x2 o = f[0] * *(LAS const f32x2*)(lds + L_OP + 2 * lane);
#pragma unroll
    for (int w = 1; w < 4; ++w) o += f[w] * *(LAS const f32x2*)(lds + L_OP + w * 128 + 2 * lane);
    const unsigned op = pack_bf16(o[0], o[1]);
    float y[8];
#pragma unroll
    for (int k = 0; k < 8; ++k) y[k] = 0.f;
#define O1(U) { const unsigned ob = dppu<BCAST0 + (U)>(op); _Pragma("unroll") for (int k = 0; k < 4; ++k) { y[k] = dot2(W[2 * (U)][k], ob, y[k]); y[4 + k] = dot2(W[2 * (U) + 1][k], ob, y[4 + k]); } }
    O1(0) O1(1) O1(2) O1(3) O1(4) O1(5) O1(6) O1(7) O1(8) O1(9) O1(10) O1(11) O1(12) O1(13) O1(14) O1(15)
#undef O1
    const f32x2 yy = fold_rows(y);
    __builtin_amdgcn_raw_buffer_store_b128(granules(yy[0], yy[1], tlo, thi), rw, yoff, 0, HS_AUX);
  }
  STAMP(5);
  if (hw != 0) { STAMP_FLUSH(); return; }
  {
    const unsigned ts = now();
    const unsigned ro = WS_Y + (grp * 64 + (lane >> 2)) * 4096 + mem * 64 + (lane & 3) * 16;
    u32x4 e[4];
    bool all;
    do {
#pragma unroll
      for (int k = 0; k < 4; ++k) e[k] = __builtin_amdgcn_raw_buffer_load_b128(rw, ro, k * 16 * 4096, HL_AUX);
      all = __all(valid(e[0], tlo, thi) && valid(e[1], tlo, thi) && valid(e[2], tlo, thi) && valid(e[3], tlo, thi));
    } while (!all && now() - ts <= SPIN_LIMIT);
    ok = ok && all;
    float s0 = 0.f, s1 = 0.f;
#pragma unroll
    for (int k = 0; k < 4; ++k) { s0 = fmaf(wgt[k], val(e[k][1]), s0); s1 = fmaf(wgt[k], val(e[k][3]), s1); }
    s0 += dpp<XOR8>(s0); s1 += dpp<XOR8>(s1);
    s0 += dpp<ROR4>(s0); s1 += dpp<ROR4>(s1);
    s0 = swap16_add(s0, s0); s1 = swap16_add(s1, s1);
    s0 = swap32_add(s0, s0); s1 = swap32_add(s1, s1);
    if (!ok) { s0 = __builtin_nanf(""); s1 = s0; }
    if (lane < 4) ((GAS unsigned*)pout)[(b * 2 * DM + prob * DM + mem * 8) / 2 + lane] = pack_bf16(s0, s1);
    if (b == 0 && mem == 0 && lane == 0) __hip_atomic_store(epw, ((unsigned long long)thi << 32) | tlo, __ATOMIC_RELAXED, __HIP_MEMORY_SCOPE_AGENT);
  }
  STAMP(6);
  STAMP_FLUSH();
}

extern "C" void kernel_launch(void* const* d_in, const int* in_sizes, int n_in, void* d_out, int out_size, void* d_ws,
                              size_t ws_size, hipStream_t stream) {
  Params P;
  for (int p = 0; p < 2; ++p) {
    P.x[p] = (const unsigned char*)d_in[5 * p + 0];
    P.k[p] = (const unsigned char*)d_in[5 * p + 1];
    P.v[p] = (const unsigned char*)d_in[5 * p + 2];
    P.wq[p] = (const unsigned char*)d_in[5 * p + 3];
    P.wo[p] = (const unsigned char*)d_in[5 * p + 4];
  }
  P.out = (unsigned char*)d_out;
  P.ws = (unsigned char*)d_ws;
  mha_decode_kernel<<<NWG, NTHR, 0, stream>>>(P);
}
```

```cpp
#include <hip/hip_runtime.h>
#include <hip/hip_bf16.h>
#include <stdint.h>

typedef unsigned u32x4 __attribute__((ext_vector_type(4)));
typedef unsigned u32x2 __attribute__((ext_vector_type(2)));
typedef float f32x4 __attribute__((ext_vector_type(4)));
typedef float f32x2 __attribute__((ext_vector_type(2)));
typedef __bf16 bf16x2 __attribute__((ext_vector_type(2)));
typedef __bf16 bf16x8 __attribute__((ext_vector_type(8)));
#define LAS __attribute__((address_space(3)))
#define GAS __attribute__((address_space(1)))

#define PACE_DQ 14150
#define PACE_DQ1 15200
#define PACE_N1 2
#define PACE_T0 130
#define EARLY 1
#define WIN 3
#define Q_DELAY 60
#define Q_MODE 0
#define WO_GAP 200
#define ADAPT 0
#define AD_TP 1000.f
#define AD_KI 0.0f
#define AD_LEAK 0.0f
#define AD_CALM 5.0f
#define AD_MODE 0
#define AD_N 16
#define AD_EVERY 1
#define CS_AUX 16
#define CL_AUX 16
#define AD_MAX 0.15f
#define WO_FINE 0
#define WO_ROT 1
#define WO_STAG 200
#define WO_T0 800
#define CODE_KB 20
#define POLL_GAP 6

constexpr int NH = 32, SK = 8192, DH = 128, DM = 512;
constexpr int NWG = 256, NTHR = 512;
constexpr unsigned REGION = 1u << 20;
constexpr int NT = 2;
#define X_AUX 2
#define HL_AUX 16
#define HS_AUX 16
constexpr float C2 = 0.08838834764831845f * 1.4426950408889634f;

constexpr unsigned LINE = 256;
constexpr unsigned WS_EG = 0, WS_EH = WS_EG + 4 * LINE, WS_ML = WS_EH + 64 * LINE, WS_CTL = WS_ML + NWG * LINE, WS_QX = WS_CTL + 2 * NWG * LINE,
                   WS_Y = WS_QX + 64 * 8192, WS_END = WS_Y + NWG * 4096;
constexpr int L_SC = 0;
constexpr int L_OP = L_SC + 4096;
constexpr int L_Q = L_OP + 512;
constexpr int L_ML = L_Q + 64;
constexpr int L_TAG = L_ML + 16;
constexpr int L_CTL = L_TAG + 2;
constexpr int L_SCL = L_TAG + 4;
constexpr int L_LATE = L_TAG + 8;
constexpr int L_WO = L_LATE + 256;
constexpr int L_END = L_WO + 32768;

struct Params {
  const unsigned char* x[2];
  const unsigned char* k[2];
  const unsigned char* v[2];
  const unsigned char* wq[2];
  const unsigned char* wo[2];
  unsigned char* out;
  unsigned char* ws;
};

__device__ __forceinline__ float dot2(unsigned a, unsigned b, float acc) {
  return __builtin_amdgcn_fdot2_f32_bf16(__builtin_bit_cast(bf16x2, a), __builtin_bit_cast(bf16x2, b), acc, false);
}
template <int CTRL> __device__ __forceinline__ unsigned dppu(unsigned x) {
  return (unsigned)__builtin_amdgcn_mov_dpp((int)x, CTRL, 0xf, 0xf, true);
}
template <int CTRL> __device__ __forceinline__ float dpp(float x) { return __builtin_bit_cast(float, dppu<CTRL>(__builtin_bit_cast(unsigned, x))); }
constexpr int XOR1 = 0xB1, XOR2 = 0x4E, XOR7 = 0x141, XOR8 = 0x128, ROR4 = 0x124, BCAST0 = 0x150;
__device__ __forceinline__ float bf_lo(unsigned u) { return __builtin_bit_cast(float, u << 16); }
__device__ __forceinline__ float bf_hi(unsigned u) { return __builtin_bit_cast(float, u & 0xffff0000u); }
__device__ __forceinline__ float swap16_add(float a, float b) {
  auto s = __builtin_amdgcn_permlane16_swap(__float_as_uint(a), __float_as_uint(b), false, false);
  return __uint_as_float(s[0]) + __uint_as_float(s[1]);
}
__device__ __forceinline__ float swap32_add(float a, float b) {
  auto s = __builtin_amdgcn_permlane32_swap(__float_as_uint(a), __float_as_uint(b), false, false);
  return __uint_as_float(s[0]) + __uint_as_float(s[1]);
}
__device__ __forceinline__ float xrow_max(float x) {
  auto s = __builtin_amdgcn_permlane16_swap(__float_as_uint(x), __float_as_uint(x), false, false);
  x = fmaxf(__uint_as_float(s[0]), __uint_as_float(s[1]));
  auto t = __builtin_amdgcn_permlane32_swap(__float_as_uint(x), __float_as_uint(x), false, false);
  return fmaxf(__uint_as_float(t[0]), __uint_as_float(t[1]));
}
__device__ __forceinline__ f32x2 fold_rows(const float (&a)[8]) {
  const float z0 = swap32_add(a[0], a[4]), z1 = swap32_add(a[1], a[5]), z2 = swap32_add(a[2], a[6]), z3 = swap32_add(a[3], a[7]);
  return f32x2{swap16_add(z0, z2), swap16_add(z1, z3)};
}
__device__ __forceinline__ unsigned pack_bf16(float lo, float hi) {
  bf16x2 r;
  r[0] = (__bf16)lo;
  r[1] = (__bf16)hi;
  return __builtin_bit_cast(unsigned, r);
}
template <int T> __device__ __forceinline__ void axpy8(float (&acc)[8], float p, const u32x4& v) {
  const float pb = dpp<BCAST0 + T>(p);
#pragma unroll
  for (int k = 0; k < 4; ++k) {
    acc[2 * k] = fmaf(pb, bf_lo(v[k]), acc[2 * k]);
    acc[2 * k + 1] = fmaf(pb, bf_hi(v[k]), acc[2 * k + 1]);
  }
}
__device__ __forceinline__ float round24(float v) { return __uint_as_float((__float_as_uint(v) + 0x80u) & 0xffffff00u); }
__device__ __forceinline__ u32x4 granules(float v0, float v1, unsigned tlo, unsigned thi) {
  return u32x4{tlo, __float_as_uint(round24(v0)) | thi, tlo, __float_as_uint(round24(v1)) | thi};
}
__device__ __forceinline__ bool valid(const u32x4& e, unsigned tlo, unsigned thi) {
  return (((e[0] ^ tlo) | (e[2] ^ tlo)) | (((e[1] ^ thi) | (e[3] ^ thi)) & 0xffu)) == 0u;
}
__device__ __forceinline__ float val(unsigned u) { return __uint_as_float(u & 0xffffff00u); }
template <int N> __device__ __forceinline__ void wait_vm() { __builtin_amdgcn_s_waitcnt((N & 15) | ((N >> 4) << 14) | (7 << 4) | (15 << 8)); }
__device__ __forceinline__ void wait_lgkm0() { __builtin_amdgcn_s_waitcnt(0xC07F); }
__device__ __forceinline__ unsigned now() { return (unsigned)__builtin_amdgcn_s_memrealtime(); }
#define PIN() __builtin_amdgcn_sched_barrier(0)
constexpr unsigned SPIN_LIMIT = 5000000u;

#define STAMP(i) do { } while (0)
#define STAMP_FLUSH() do { } while (0)

__global__ void __launch_bounds__(NTHR, 2) mha_decode_kernel(Params P) {
  __shared__ __attribute__((aligned(16))) float lds_raw[L_END];
  LAS float* lds = (LAS float*)lds_raw;
  STAMP(0);
  unsigned t0 = now();
  PIN();
  const int bid = blockIdx.x;
  const int prob = bid >> 7, h = (bid & 7) * 4 + ((bid >> 5) & 3), sub = (bid >> 3) & 3;
  const int ph = prob * 32 + h, b = sub >> 1, split = sub & 1;
  const int lane = threadIdx.x & 63;
  const int wave = __builtin_amdgcn_readfirstlane(threadIdx.x >> 6);
  const int g = lane >> 4, c = lane & 15;
  const unsigned mem = 2 * h + split;
  const unsigned grp = 2 * prob + b;

  const unsigned char *px = prob ? P.x[1] : P.x[0], *pk = prob ? P.k[1] : P.k[0], *pv = prob ? P.v[1] : P.v[0];
  const unsigned char *pwq = prob ? P.wq[1] : P.wq[0], *pwo = prob ? P.wo[1] : P.wo[0];
  unsigned char *pout = P.out, *pws = P.ws;
  asm volatile("" : "+s"(px), "+s"(pk), "+s"(pv), "+s"(pwq), "+s"(pwo), "+s"(pout), "+s"(pws));

  const unsigned region = b * 64 + mem;
  const auto rk = __builtin_amdgcn_make_buffer_rsrc((void*)(pk + (size_t)region * REGION), 0, (int)REGION, 0x00020000);
  const unsigned voff = lane * 16 + ((wave & 1) << 13) + ((wave & 2) << 14);
  unsigned pos = mem;
  unsigned tq = (t0 + PACE_T0) << 8;
  unsigned dq = PACE_DQ1;
  u32x4 R[12][4];
  auto issue = [&](u32x4 (&dst)[4], const __amdgpu_buffer_rsrc_t& r) {
    const unsigned s = ((pos & 4u) << 10) | ((pos & 1u) << 14) | ((pos & 2u) << 15) | ((pos & 56u) << 14);
#pragma unroll
    for (int t = 0; t < 4; ++t) dst[t] = __builtin_amdgcn_raw_buffer_load_b128(r, voff + t * 1024, s, NT);
    pos += 1;
    tq += dq;
    PIN();
  };
  if (wave < 4) {
#pragma unroll
    for (int j = 0; j < EARLY; ++j) issue(R[j], rk);
  }
  int role = wave;
  asm volatile("" : "+s"(role));
  if (role == 5) {
    unsigned long long pc;
    asm volatile("s_getpc_b64 %0" : "=s"(pc));
    const unsigned slice = (unsigned)(bid >> 3) % CODE_KB;
    const u32x4 v = *(const GAS u32x4*)((pc & ~255ull) + slice * 1024 + lane * 16);
    asm volatile("" :: "v"(v));
  }

  GAS unsigned long long* epg = (GAS unsigned long long*)(pws + WS_EG + grp * LINE);
  const auto rw = __builtin_amdgcn_make_buffer_rsrc((void*)pws, 0, (int)WS_END, 0x00020000);
  bool ok = true;
  if (role == 4) {
    const auto rq = __builtin_amdgcn_make_buffer_rsrc((void*)(pwq + (size_t)sub * 128 * (NH * DH * 2) + h * (DH * 2)), 0, 128 * NH * DH * 2 - h * (DH * 2), 0x00020000);
    const auto rx = __builtin_amdgcn_make_buffer_rsrc((void*)px, 0, 2 * DM * 2, 0x00020000);
    const unsigned xo = (128 * sub + 4 * c + g) * 2;
    const unsigned x00 = __builtin_amdgcn_raw_buffer_load_b16(rx, xo, 0, X_AUX), x01 = __builtin_amdgcn_raw_buffer_load_b16(rx, xo + DM * 2, 0, X_AUX);
    const unsigned x10 = __builtin_amdgcn_raw_buffer_load_b16(rx, xo + 128, 0, X_AUX), x11 = __builtin_amdgcn_raw_buffer_load_b16(rx, xo + 128 + DM * 2, 0, X_AUX);
    u32x4 W[32];
    PIN();
    const unsigned vo = g * (NH * DH * 2) + c * 16;
#pragma unroll
    for (int t = 0; t < 32; ++t) W[t] = __builtin_amdgcn_raw_buffer_load_b128(rq, vo, t * 4 * (NH * DH * 2), NT);
    GAS unsigned long long* eph = (GAS unsigned long long*)(pws + WS_EH + ph * LINE);
    const unsigned long long prev = __hip_atomic_load(eph, __ATOMIC_RELAXED, __HIP_MEMORY_SCOPE_AGENT);
    PIN();
    STAMP(7);
    if (lane < 4) *(volatile LAS unsigned*)(lds + L_ML + 4 * lane + 2) = 0u;
    const unsigned xa = x00 | (x01 << 16), xb = x10 | (x11 << 16);
    unsigned sel[4];
#pragma unroll
    for (int k = 0; k < 4; ++k) {
      const unsigned pick = (c & 8) ? 0x0302u : 0x0100u;
      sel[k] = ((c & 7) == 2 * k ? pick : 0x0c0cu) | (((c & 7) == 2 * k + 1 ? pick : 0x0c0cu) << 16);
    }
    f32x4 d0 = {0.f, 0.f, 0.f, 0.f}, d1 = {0.f, 0.f, 0.f, 0.f};
#define QM(T, X, D) { const unsigned xt = dppu<BCAST0 + ((T) & 15)>(X); \
      const u32x4 a = {__builtin_amdgcn_perm(xt, xt, sel[0]), __builtin_amdgcn_perm(xt, xt, sel[1]), __builtin_amdgcn_perm(xt, xt, sel[2]), __builtin_amdgcn_perm(xt, xt, sel[3])}; \
      D = __builtin_amdgcn_mfma_f32_16x16x32_bf16(__builtin_bit_cast(bf16x8, a), __builtin_bit_cast(bf16x8, W[T]), D, 0, 0, 0); }
#define QM4(T, X) QM(T, X, d0) QM(T + 1, X, d1) QM(T + 2, X, d0) QM(T + 3, X, d1) PIN();
    QM4(0, xa) QM4(4, xa) QM4(8, xa) QM4(12, xa) QM4(16, xb) QM4(20, xb) QM4(24, xb) QM4(28, xb)
#undef QM4
#undef QM
    d0 += d1;
    const unsigned long long tg = prev + 1ull;
    const unsigned tlo = __builtin_amdgcn_readfirstlane((unsigned)tg), thi = __builtin_amdgcn_readfirstlane((unsigned)(tg >> 32)) & 0xffu;
    STAMP(8);
    const unsigned qo = WS_QX + ph * 8192 + sub * 2048 + (g >> 1) * 1024 + c * 64 + (g & 1) * 32;
    __builtin_amdgcn_raw_buffer_store_b128(granules(d0[0], d0[1], tlo, thi), rw, qo, 0, HS_AUX);
    __builtin_amdgcn_raw_buffer_store_b128(granules(d0[2], d0[3], tlo, thi), rw, qo + 16, 0, HS_AUX);
    STAMP(9);
    const unsigned ts = now();
    while (now() - ts < Q_DELAY) __builtin_amdgcn_s_sleep(1);
    u32x4 e[4];
    do {
#pragma unroll
      for (int k = 0; k < 4; ++k) e[k] = __builtin_amdgcn_raw_buffer_load_b128(rw, WS_QX + ph * 8192 + b * 1024 + lane * 16, k * 2048, HL_AUX);
      ok = __all((int)valid(e[0], tlo, thi) & (int)valid(e[1], tlo, thi) & (int)valid(e[2], tlo, thi) & (int)valid(e[3], tlo, thi));
    } while (!ok && now() - ts <= SPIN_LIMIT);
    STAMP(10);
    *(LAS unsigned*)(lds + L_Q + lane) = pack_bf16((val(e[0][1]) + val(e[1][1])) + (val(e[2][1]) + val(e[3][1])), (val(e[0][3]) + val(e[1][3])) + (val(e[2][3]) + val(e[3][3])));
    if (sub == 0 && lane == 0 && ok) __hip_atomic_store(eph, ((unsigned long long)thi << 32) | tlo, __ATOMIC_RELAXED, __HIP_MEMORY_SCOPE_AGENT);
    wait_lgkm0();
  }
  if (role == 5) {
    while (now() - t0 < 100u + mem * 8u) __builtin_amdgcn_s_sleep(2);
    const unsigned long long tg = __hip_atomic_load(epg, __ATOMIC_RELAXED, __HIP_MEMORY_SCOPE_AGENT) + 1ull;
    if (lane == 0) { *(LAS u32x4*)(lds + L_TAG) = u32x4{(unsigned)tg, (unsigned)(tg >> 32) & 0xffu, PACE_DQ, 0u}; *(LAS u32x2*)(lds + L_SCL) = u32x2{0u, 0u}; }
    wait_lgkm0();
  }
  asm volatile("" : "+s"(role));

  if (role < 4) {
    __builtin_amdgcn_s_setprio(3);
    const auto rv = __builtin_amdgcn_make_buffer_rsrc((void*)(pv + (size_t)region * REGION), 0, (int)REGION, 0x00020000);
    auto gate = [&]() {
      PIN();
      wait_vm<4 * (WIN - 1)>();
      while ((int)((now() << 8) - tq) < 0) __builtin_amdgcn_s_sleep(1);
      PIN();
    };
    auto gate_ctl = [&](bool slow) {
      PIN();
      wait_vm<4 * (WIN - 1)>();
      dq = slow ? PACE_DQ1 : PACE_DQ;
      while ((int)((now() << 8) - tq) < 0) __builtin_amdgcn_s_sleep(1);
      PIN();
    };
    STAMP(6);
#pragma unroll
    for (int j = EARLY; j < 12; ++j) { gate(); if (j == 2) STAMP(7); if (j == 3) STAMP(8); if (j == 7) STAMP(9); issue(R[j], rk); }
    STAMP(1);
    __builtin_amdgcn_s_barrier();
    PIN();
    const u32x4 q = *(LAS const u32x4*)(lds + L_Q + 4 * c);
    STAMP(2);
    LAS float* sc = lds + L_SC + wave * 1024 + lane;
    float keep = 0.f, mx = -INFINITY;
    auto score = [&](const u32x4 (&K)[4], int J, int row) {
      float x[4];
#pragma unroll
      for (int t = 0; t < 4; ++t) {
        float d = dot2(K[t][0], q[0], 0.f);
        d = dot2(K[t][1], q[1], d);
        d = dot2(K[t][2], q[2], d);
        x[t] = dot2(K[t][3], q[3], d);
      }
      const float a0 = x[0] + dpp<XOR8>(x[0]), a1 = x[1] + dpp<XOR8>(x[1]), a2 = x[2] + dpp<XOR8>(x[2]), a3 = x[3] + dpp<XOR8>(x[3]);
      const float y0 = (c & 8) ? a2 : a0, y1 = (c & 8) ? a3 : a1;
      const float b0 = y0 + dpp<XOR7>(y0), b1 = y1 + dpp<XOR7>(y1);
      float s = (c & 4) ? b1 : b0;
      s += dpp<XOR2>(s);
      s += dpp<XOR1>(s);
      keep = ((c & 3) == J) ? s : keep;
      if (J == 3) { sc[row * 64] = keep; mx = fmaxf(mx, keep); }
      asm volatile("" : "+v"(keep), "+v"(mx));
    };
    score(R[0], 0, 0); score(R[1], 1, 0); score(R[2], 2, 0); score(R[3], 3, 0);
    gate(); issue(R[0], rk);
    score(R[4], 0, 1); score(R[5], 1, 1); score(R[6], 2, 1); score(R[7], 3, 1);
    gate(); issue(R[1], rk);
    score(R[8], 0, 2); score(R[9], 1, 2); score(R[10], 2, 2);
    gate(); issue(R[2], rk);
    score(R[11], 3, 2);
    if (WIN == 2) score(R[0], 0, 3);
    gate(); issue(R[3], rk);
    score(R[3 - WIN], 3 - WIN, 3);
#pragma unroll 1
    for (int it = 0; it < 12; ++it) {
#pragma unroll
      for (int j = 0; j < 4; ++j) {
        if (j == 0) gate_ctl(it < PACE_N1); else gate();
        issue(R[j], rk);
        score(R[(j + 4 - WIN) & 3], (j + 4 - WIN) & 3, j < WIN ? 3 + it : 4 + it);
      }
    }
#pragma unroll
    for (int j = 0; j < WIN; ++j) {
      gate();
      issue(R[j], rv);
      score(R[4 - WIN + j], 4 - WIN + j, 15);
    }
    mx = fmaxf(mx, dpp<XOR1>(mx)); mx = fmaxf(mx, dpp<XOR2>(mx)); mx = fmaxf(mx, dpp<XOR7>(mx)); mx = fmaxf(mx, dpp<XOR8>(mx));
    mx = xrow_max(mx);
    const float m2 = mx * C2;
    {
      float l = 0.f;
#pragma unroll
      for (int u = 0; u < 16; ++u) {
        const float p = __builtin_amdgcn_exp2f(fmaf(sc[u * 64], C2, -m2));
        sc[u * 64] = p;
        l += p;
      }
      l += dpp<XOR1>(l); l += dpp<XOR2>(l); l += dpp<XOR7>(l); l += dpp<XOR8>(l);
      l = swap16_add(l, l);
      l = swap32_add(l, l);
      if (lane == 0) {
        *(LAS f32x2*)(lds + L_ML + 4 * wave) = f32x2{m2, l};
        wait_lgkm0();
        *(volatile LAS unsigned*)(lds + L_ML + 4 * wave + 2) = 1u;
      }
    }
    STAMP(3);
    float acc[8];
#pragma unroll
    for (int k = 0; k < 8; ++k) acc[k] = 0.f;
#define PV(RR, J) do { axpy8<0 + J>(acc, p, RR[0]); axpy8<4 + J>(acc, p, RR[1]); axpy8<8 + J>(acc, p, RR[2]); axpy8<12 + J>(acc, p, RR[3]); \
    asm volatile("" : "+v"(acc[0]), "+v"(acc[1]), "+v"(acc[2]), "+v"(acc[3]), "+v"(acc[4]), "+v"(acc[5]), "+v"(acc[6]), "+v"(acc[7])); } while (0)
    float p = sc[0];
#pragma unroll 1
    for (int u = 0; u < 15; ++u) {
      const float pn = sc[(u + 1) * 64];
      gate_ctl(false); issue(R[(0 + WIN) & 3], rv); PV(R[0], 0);
      gate(); issue(R[(1 + WIN) & 3], rv); PV(R[1], 1);
      gate(); issue(R[(2 + WIN) & 3], rv); PV(R[2], 2);
      gate(); issue(R[(3 + WIN) & 3], rv); PV(R[3], 3);
      p = pn;
    }
    gate(); issue(R[(0 + WIN) & 3], rv);
    if (WIN == 2) { PV(R[0], 0); gate(); issue(R[3], rv); }
    STAMP(4);
    PIN();
    if (WIN == 3) PV(R[0], 0);
    PV(R[1], 1);
    PV(R[2], 2);
    PV(R[3], 3);
#undef PV
    *(LAS f32x2*)(lds + L_OP + wave * 128 + 2 * lane) = fold_rows(acc);
    wait_lgkm0();
    STAMP(5);
    __builtin_amdgcn_s_barrier();
    STAMP_FLUSH();
    return;
  }

  const int hw = wave - 4;
  u32x4 W[32];
  STAMP(1);
  __builtin_amdgcn_s_barrier();
  PIN();
  const u32x2 tg2 = *(LAS const u32x2*)(lds + L_TAG);
  const unsigned tlo = __builtin_amdgcn_readfirstlane(tg2[0]), thi = __builtin_amdgcn_readfirstlane(tg2[1]);
  const unsigned yoff = WS_Y + (grp * 64 + mem) * 4096 + hw * 1024 + c * 64 + g * 16;
  const auto ro = __builtin_amdgcn_make_buffer_rsrc((void*)pwo, 0, DM * NH * DH * 2, 0x00020000);
  const unsigned wvo = (unsigned)(h * DH + 2 * g) * (DM * 2) + hw * 256 + c * 16;
  const unsigned gath = WS_Y + (grp * 64 + (lane >> 2)) * 4096 + mem * 64 + (lane & 3) * 16;
  unsigned tw = t0 + WO_T0 + (unsigned)(((h * 2 + prob) * WO_STAG) >> 6);
  auto wsum = [&](float a) { a += dpp<XOR1>(a); a += dpp<XOR2>(a); a += dpp<XOR7>(a); a += dpp<XOR8>(a); a = swap16_add(a, a); return swap32_add(a, a); };
  auto stats = [&](float (&f)[4], float& m, float& l) {
    const unsigned ts = now();
    for (;;) {
      const unsigned fl = *(volatile LAS unsigned*)(lds + L_ML + 4 * (lane & 3) + 2);
      if (__all(fl == 1u) || now() - ts > SPIN_LIMIT) break;
      __builtin_amdgcn_s_sleep(16);
    }
    asm volatile("" ::: "memory");
    float mw[4], lw[4];
#pragma unroll
    for (int w = 0; w < 4; ++w) { const f32x2 ml = *(volatile LAS f32x2*)(lds + L_ML + 4 * w); mw[w] = ml[0]; lw[w] = ml[1]; }
    m = round24(fmaxf(fmaxf(mw[0], mw[1]), fmaxf(mw[2], mw[3])));
    l = 0.f;
#pragma unroll
    for (int w = 0; w < 4; ++w) { f[w] = __builtin_amdgcn_exp2f(mw[w] - m); l = fmaf(f[w], lw[w], l); }
  };
  float f[4];
  float c_int = 0.f;
#pragma unroll 1
  for (int t = 0; t < 32; ++t) {
    while ((int)(now() - tw) < -WO_FINE) __builtin_amdgcn_s_sleep(8);
    while ((int)(now() - tw) < 0) { }
    tw += WO_GAP;
    {
      const int pc = (t + WO_ROT * (h + 16 * prob)) & 31;
      __builtin_amdgcn_raw_ptr_buffer_load_lds(ro, (LAS void*)(lds + L_WO + (hw * 32 + pc) * 256), 16, wvo, ((pc >> 1) * 8 + (pc & 1)) * (DM * 2), 0, NT);
    }
    if (ADAPT && hw == 1) {
      float a = (float)*(volatile LAS int*)(lds + L_LATE + (lane & 3) * 64);
      a += dpp<XOR1>(a); a += dpp<XOR2>(a);
      const u32x4 gr = granules(a * (0.25f / 256.f), 0.f, tlo, thi ^ t);
      if (lane == 0 && t % AD_EVERY == 0) __builtin_amdgcn_raw_buffer_store_b64(u32x2{gr[0], gr[1]}, rw, WS_CTL + (((t / AD_EVERY) & 1) * NWG + bid) * LINE, 0, CS_AUX);
      if (t > 0 && (t - 1) % AD_EVERY == 0) {
        const unsigned src = (bid & ~0x67) | (lane & 7) | ((((bid >> 5) + (lane >> 3)) & 3) << 5);
        const u32x2 e = __builtin_amdgcn_raw_buffer_load_b64(rw, lane < AD_N ? WS_CTL + ((((t - 1) / AD_EVERY) & 1) * NWG + src) * LINE : WS_END, 0, CL_AUX);
        const bool v0 = e[0] == tlo && (e[1] & 0xffu) == (thi ^ (t - 1)) && lane < AD_N;
        const float l0 = v0 ? val(e[1]) : 0.f;
        const float n = wsum(v0 ? 1.f : 0.f), s1 = wsum(l0);
        const float err = AD_MODE ? wsum(l0 * l0) / (s1 + 8.f * n + 1.f) : s1 / fmaxf(n, 1.f);
        if (n >= 0.5f * AD_N) {
          const float pterm = err * (1.f / AD_TP);
          c_int = err < AD_CALM ? fmaxf(c_int - AD_LEAK, 0.f) : fminf(c_int + AD_KI * pterm, AD_MAX);
          if (lane == 0) *(volatile LAS unsigned*)(lds + L_CTL) = (unsigned)((float)PACE_DQ * (1.f + fminf(c_int + pterm, AD_MAX)));
        }
      }
    }
    if (t == 8 && hw == 2) {
      const unsigned char* a = lane == 0 ? pv + (size_t)region * REGION : lane == 1 ? pout : pws + WS_Y + grp * 64 * 4096 + (lane & 3) * 65536;
      const unsigned v = __builtin_nontemporal_load((const GAS unsigned*)a);
      asm volatile("" :: "v"(v));
    }
    if (t == 16 && hw == 0) {
      float m, l;
      stats(f, m, l);
      if (lane == 0) __builtin_amdgcn_raw_buffer_store_b128(granules(m, l, tlo, thi), rw, WS_ML + bid * LINE, 0, HS_AUX);
    }
    if (t == 24 && hw == 0) {
      const unsigned ts = now();
      u32x4 e;
      for (;;) {
        e = __builtin_amdgcn_raw_buffer_load_b128(rw, WS_ML + (bid ^ ((lane & 1) << 3)) * LINE, 0, HL_AUX);
        if (__all(valid(e, tlo, thi))) break;
        if (now() - ts > SPIN_LIMIT) { ok = false; break; }
        __builtin_amdgcn_s_sleep(64);
      }
      const float m0 = val(e[1]), l0 = val(e[3]);
      const float m1 = dpp<XOR1>(m0), l1 = dpp<XOR1>(l0);
      const float M = fmaxf(m0, m1);
      const float e0 = __builtin_amdgcn_exp2f(m0 - M), e1 = __builtin_amdgcn_exp2f(m1 - M);
      const float wv = ok ? e0 / fmaf(e0, l0, e1 * l1) : __builtin_nanf("");
      if (lane == 0) {
        *(volatile LAS float*)(lds + L_SCL) = wv;
        wait_lgkm0();
        *(volatile LAS unsigned*)(lds + L_SCL + 1) = 1u;
      }
    }
  }
  STAMP(2);
  {
    float m, l;
    stats(f, m, l);
    const unsigned ts = now();
    while (*(volatile LAS unsigned*)(lds + L_SCL + 1) != 1u && now() - ts <= SPIN_LIMIT) __builtin_amdgcn_s_sleep(4);
    const float scl = *(volatile LAS float*)(lds + L_SCL);
#pragma unroll
    for (int w = 0; w < 4; ++w) f[w] *= scl;
  }
  asm volatile("s_waitcnt vmcnt(0)" ::: "memory");
#pragma unroll
  for (int t = 0; t < 32; ++t) W[t] = *(LAS const u32x4*)(lds + L_WO + (hw * 32 + t) * 256 + lane * 4);
#pragma unroll
  for (int u = 0; u < 16; ++u) {
    const u32x4 r0 = W[2 * u], r1 = W[2 * u + 1];
#pragma unroll
    for (int k = 0; k < 2; ++k) {
      W[2 * u][2 * k] = __builtin_amdgcn_perm(r1[k], r0[k], 0x05040100u);
      W[2 * u][2 * k + 1] = __builtin_amdgcn_perm(r1[k], r0[k], 0x07060302u);
      W[2 * u + 1][2 * k] = __builtin_amdgcn_perm(r1[k + 2], r0[k + 2], 0x05040100u);
      W[2 * u + 1][2 * k + 1] = __builtin_amdgcn_perm(r1[k + 2], r0[k + 2], 0x07060302u);
    }
  }
#pragma unroll
  for (int t = 0; t < 32; ++t) asm volatile("" : "+v"(W[t]));
  STAMP(3);
  __builtin_amdgcn_s_setprio(3);
  __builtin_amdgcn_s_barrier();
  PIN();
  STAMP(4);
  {
    f32x2 o = f[0] * *(LAS const f32x2*)(lds + L_OP + 2 * lane);
#pragma unroll
    for (int w = 1; w < 4; ++w) o += f[w] * *(LAS const f32x2*)(lds + L_OP + w * 128 + 2 * lane);
    const unsigned op = pack_bf16(o[0], o[1]);
    float y[8];
#pragma unroll
    for (int k = 0; k < 8; ++k) y[k] = 0.f;
#define O1(U) { const unsigned ob = dppu<BCAST0 + (U)>(op); _Pragma("unroll") for (int k = 0; k < 4; ++k) { y[k] = dot2(W[2 * (U)][k], ob, y[k]); y[4 + k] = dot2(W[2 * (U) + 1][k], ob, y[4 + k]); } }
    O1(0) O1(1) O1(2) O1(3) O1(4) O1(5) O1(6) O1(7) O1(8) O1(9) O1(10) O1(11) O1(12) O1(13) O1(14) O1(15)
#undef O1
    const f32x2 yy = fold_rows(y);
    __builtin_amdgcn_raw_buffer_store_b128(granules(yy[0], yy[1], tlo, thi), rw, yoff, 0, HS_AUX);
  }
  STAMP(5);
  if (hw != 0) { STAMP_FLUSH(); return; }
  {
    u32x4 e[6][4];
    float s0 = 0.f, s1 = 0.f;
    auto fetch = [&](u32x4 (&d)[4]) {
#pragma unroll
      for (int k = 0; k < 4; ++k) d[k] = __builtin_amdgcn_raw_buffer_load_b128(rw, gath, k * 16 * 4096, HL_AUX);
      PIN();
    };
    auto take = [&](const u32x4 (&d)[4]) {
      s0 = (val(d[0][1]) + val(d[1][1])) + (val(d[2][1]) + val(d[3][1]));
      s1 = (val(d[0][3]) + val(d[1][3])) + (val(d[2][3]) + val(d[3][3]));
      return (bool)__all((int)valid(d[0], tlo, thi) & (int)valid(d[1], tlo, thi) & (int)valid(d[2], tlo, thi) & (int)valid(d[3], tlo, thi));
    };
    fetch(e[0]); __builtin_amdgcn_s_sleep(POLL_GAP);
    fetch(e[1]); __builtin_amdgcn_s_sleep(POLL_GAP);
    fetch(e[2]);
    bool all = take(e[0]);
    if (!all) { fetch(e[3]); all = take(e[1]);
      if (!all) { fetch(e[4]); all = take(e[2]);
        if (!all) { fetch(e[5]); all = take(e[3]);
          if (!all) { all = take(e[4]);
            if (!all) {
              const unsigned ts = now();
              bool v[4];
#pragma unroll
              for (int k = 0; k < 4; ++k) v[k] = valid(e[5][k], tlo, thi);
              while (!(all = take(e[5])) && now() - ts <= SPIN_LIMIT) {
                __builtin_amdgcn_s_sleep(4);
#pragma unroll
                for (int k = 0; k < 4; ++k) e[4][k] = __builtin_amdgcn_raw_buffer_load_b128(rw, v[k] ? WS_END : gath + k * 16 * 4096, 0, HL_AUX);
#pragma unroll
                for (int k = 0; k < 4; ++k) { if (!v[k]) e[5][k] = e[4][k]; v[k] = valid(e[5][k], tlo, thi); }
              }
            } } } } }
    ok = ok && all;
    s0 += dpp<XOR8>(s0); s1 += dpp<XOR8>(s1);
    s0 += dpp<ROR4>(s0); s1 += dpp<ROR4>(s1);
    s0 = swap16_add(s0, s0); s1 = swap16_add(s1, s1);
    s0 = swap32_add(s0, s0); s1 = swap32_add(s1, s1);
    if (!ok) { s0 = __builtin_nanf(""); s1 = s0; }
    if (lane < 4) ((GAS unsigned*)pout)[(b * 2 * DM + prob * DM + mem * 8) / 2 + lane] = pack_bf16(s0, s1);
    STAMP(6);
    if (mem == 0 && lane == 0 && all) __hip_atomic_store(epg, ((unsigned long long)thi << 32) | tlo, __ATOMIC_RELAXED, __HIP_MEMORY_SCOPE_AGENT);
    PIN();
#pragma unroll
    for (int i = 0; i < 6; ++i) asm volatile("" :: "v"(e[i][0]), "v"(e[i][1]), "v"(e[i][2]), "v"(e[i][3]));
  }
  STAMP(11);
  STAMP_FLUSH();
}

extern "C" void kernel_launch(void* const* d_in, const int* in_sizes, int n_in, void* d_out, int out_size, void* d_ws,
                              size_t ws_size, hipStream_t stream) {
  Params P;
  for (int p = 0; p < 2; ++p) {
    P.x[p] = (const unsigned char*)d_in[5 * p + 0];
    P.k[p] = (const unsigned char*)d_in[5 * p + 1];
    P.v[p] = (const unsigned char*)d_in[5 * p + 2];
    P.wq[p] = (const unsigned char*)d_in[5 * p + 3];
    P.wo[p] = (const unsigned char*)d_in[5 * p + 4];
  }
  P.out = (unsigned char*)d_out;
  P.ws = (unsigned char*)d_ws;
  mha_decode_kernel<<<NWG, NTHR, 0, stream>>>(P);
}
```

```cpp
#include <hip/hip_runtime.h>
#include <hip/hip_bf16.h>
#include <stdint.h>

typedef unsigned u32x2 __attribute__((ext_vector_type(2)));
typedef unsigned u32x4 __attribute__((ext_vector_type(4)));
typedef unsigned u32x16 __attribute__((ext_vector_type(16)));
typedef float f32x2 __attribute__((ext_vector_type(2)));
typedef float f32x4 __attribute__((ext_vector_type(4)));
typedef __bf16 bf16x2 __attribute__((ext_vector_type(2)));
typedef __bf16 bf16x8 __attribute__((ext_vector_type(8)));
#define LAS __attribute__((address_space(3)))
#define GAS __attribute__((address_space(1)))

#define PACE_DQ 14120
#define PACE_DQ1 15200
#define PACE_N1 2
#define PACE_T0 130
#define WIN 3
#define Q_DELAY 200
#define WO_T0 800
#define WO_GAP 196
#define POLL_T0 14
#define POLL_GAP 6
#define CODE_SLICE 688
#define ADAPT 1
#define AD_LGP 8
#define AD_D 180
#define AD_T0 1100
#define AD_TP 1000.f
#define AD_KI 0.0f
#define AD_LEAK 0.0f
#define AD_CALM 10.0f
#define AD_MODE 2
#define AD_MAX 0.2f

constexpr int NH = 32, DH = 128, DM = 512;
constexpr int NWG = 256, NTHR = 512;
constexpr unsigned REGION = 1u << 20;
constexpr int NT = 2, SC1 = 16;
constexpr float C2 = 0.08838834764831845f * 1.4426950408889634f;

constexpr unsigned LINE = 256;
constexpr unsigned WS_EG = 0, WS_EH = WS_EG + 4 * LINE, WS_ML = WS_EH + 64 * LINE, WS_CTL = WS_ML + NWG * LINE, WS_QX = WS_CTL + 256 * LINE,
                   WS_Y = WS_QX + 64 * 8192, WS_END = WS_Y + NWG * 4096;
constexpr int L_SC = 0;
constexpr int L_OP = L_SC + 4096;
constexpr int L_Q = L_OP + 512;
constexpr int L_ML = L_Q + 64;
constexpr int L_TAG = L_ML + 16;
constexpr int L_CTL = L_TAG + 2;
constexpr int L_SCL = L_TAG + 4;
constexpr int L_LATE = L_TAG + 8;
constexpr int L_WO = L_LATE + 256;
constexpr int L_END = L_WO + 32768;

struct Params {
  const unsigned char* x[2];
  const unsigned char* k[2];
  const unsigned char* v[2];
  const unsigned char* wq[2];
  const unsigned char* wo[2];
  unsigned char* out;
  unsigned char* ws;
};

__device__ __forceinline__ float dot2(unsigned a, unsigned b, float acc) {
  return __builtin_amdgcn_fdot2_f32_bf16(__builtin_bit_cast(bf16x2, a), __builtin_bit_cast(bf16x2, b), acc, false);
}
template <int CTRL> __device__ __forceinline__ unsigned dppu(unsigned x) { return (unsigned)__builtin_amdgcn_mov_dpp((int)x, CTRL, 0xf, 0xf, true); }
template <int CTRL> __device__ __forceinline__ float dpp(float x) { return __builtin_bit_cast(float, dppu<CTRL>(__builtin_bit_cast(unsigned, x))); }
constexpr int XOR1 = 0xB1, XOR2 = 0x4E, XOR7 = 0x141, XOR8 = 0x128, ROR4 = 0x124, BCAST0 = 0x150;
__device__ __forceinline__ float bf_lo(unsigned u) { return __builtin_bit_cast(float, u << 16); }
__device__ __forceinline__ float bf_hi(unsigned u) { return __builtin_bit_cast(float, u & 0xffff0000u); }
__device__ __forceinline__ float swap16_add(float a, float b) {
  auto s = __builtin_amdgcn_permlane16_swap(__float_as_uint(a), __float_as_uint(b), false, false);
  return __uint_as_float(s[0]) + __uint_as_float(s[1]);
}
__device__ __forceinline__ float swap32_add(float a, float b) {
  auto s = __builtin_amdgcn_permlane32_swap(__float_as_uint(a), __float_as_uint(b), false, false);
  return __uint_as_float(s[0]) + __uint_as_float(s[1]);
}
__device__ __forceinline__ float xrow_max(float x) {
  auto s = __builtin_amdgcn_permlane16_swap(__float_as_uint(x), __float_as_uint(x), false, false);
  x = fmaxf(__uint_as_float(s[0]), __uint_as_float(s[1]));
  auto t = __builtin_amdgcn_permlane32_swap(__float_as_uint(x), __float_as_uint(x), false, false);
  return fmaxf(__uint_as_float(t[0]), __uint_as_float(t[1]));
}
__device__ __forceinline__ f32x2 fold_rows(const float (&a)[8]) {
  const float z0 = swap32_add(a[0], a[4]), z1 = swap32_add(a[1], a[5]), z2 = swap32_add(a[2], a[6]), z3 = swap32_add(a[3], a[7]);
  return f32x2{swap16_add(z0, z2), swap16_add(z1, z3)};
}
__device__ __forceinline__ unsigned pack_bf16(float lo, float hi) {
  bf16x2 r;
  r[0] = (__bf16)lo;
  r[1] = (__bf16)hi;
  return __builtin_bit_cast(unsigned, r);
}
template <int T> __device__ __forceinline__ void axpy8(float (&acc)[8], float p, const u32x4& v) {
  const float pb = dpp<BCAST0 + T>(p);
#pragma unroll
  for (int k = 0; k < 4; ++k) {
    acc[2 * k] = fmaf(pb, bf_lo(v[k]), acc[2 * k]);
    acc[2 * k + 1] = fmaf(pb, bf_hi(v[k]), acc[2 * k + 1]);
  }
}
__device__ __forceinline__ float round24(float v) { return __uint_as_float((__float_as_uint(v) + 0x80u) & 0xffffff00u); }
__device__ __forceinline__ u32x4 granules(float v0, float v1, unsigned tlo, unsigned thi) {
  return u32x4{tlo, __float_as_uint(round24(v0)) | thi, tlo, __float_as_uint(round24(v1)) | thi};
}
__device__ __forceinline__ bool valid(const u32x4& e, unsigned tlo, unsigned thi) {
  return (((e[0] ^ tlo) | (e[2] ^ tlo)) | (((e[1] ^ thi) | (e[3] ^ thi)) & 0xffu)) == 0u;
}
__device__ __forceinline__ bool valid4(const u32x4 (&e)[4], unsigned tlo, unsigned thi) {
  return __all((int)valid(e[0], tlo, thi) & (int)valid(e[1], tlo, thi) & (int)valid(e[2], tlo, thi) & (int)valid(e[3], tlo, thi));
}
__device__ __forceinline__ float val(unsigned u) { return __uint_as_float(u & 0xffffff00u); }
template <int N> __device__ __forceinline__ void wait_vm() { __builtin_amdgcn_s_waitcnt((N & 15) | ((N >> 4) << 14) | (7 << 4) | (15 << 8)); }
__device__ __forceinline__ void wait_lgkm0() { __builtin_amdgcn_s_waitcnt(0xC07F); }
__device__ __forceinline__ unsigned now() { return (unsigned)__builtin_amdgcn_s_memrealtime(); }
__device__ __forceinline__ u32x2 sload2(const unsigned char* p) {
  u32x2 e;
  asm volatile("s_load_dwordx2 %0, %1, 0x0\n\ts_waitcnt lgkmcnt(0)" : "=&s"(e) : "s"(p) : "memory");
  return e;
}
__device__ __forceinline__ u32x16 sload16(const unsigned char* p) {
  u32x16 e;
  asm volatile("s_load_dwordx16 %0, %1, 0x0\n\ts_waitcnt lgkmcnt(0)" : "=&s"(e) : "s"(p) : "memory");
  return e;
}
#define PIN() __builtin_amdgcn_sched_barrier(0)
constexpr unsigned SPIN_LIMIT = 5000000u;

#define STAMP(i) do { } while (0)
#define STAMP_FLUSH() do { } while (0)

__global__ void __launch_bounds__(NTHR, 2) mha_decode_kernel(Params P) {
  __shared__ __attribute__((aligned(16))) float lds_raw[L_END];
  LAS float* lds = (LAS float*)lds_raw;
  STAMP(0);
  const unsigned t0 = now();
  PIN();
  const int bid = blockIdx.x;
  const int prob = bid >> 7, h = (bid & 7) * 4 + ((bid >> 5) & 3), sub = (bid >> 3) & 3;
  const int ph = prob * 32 + h, b = sub >> 1, split = sub & 1;
  const int lane = threadIdx.x & 63;
  const int wave = __builtin_amdgcn_readfirstlane(threadIdx.x >> 6);
  const int g = lane >> 4, c = lane & 15;
  const unsigned mem = 2 * h + split;
  const unsigned grp = 2 * prob + b;

  const unsigned char *px = prob ? P.x[1] : P.x[0], *pk = prob ? P.k[1] : P.k[0], *pv = prob ? P.v[1] : P.v[0];
  const unsigned char *pwq = prob ? P.wq[1] : P.wq[0], *pwo = prob ? P.wo[1] : P.wo[0];
  unsigned char *pout = P.out, *pws = P.ws;
  asm volatile("" : "+s"(px), "+s"(pk), "+s"(pv), "+s"(pwq), "+s"(pwo), "+s"(pout), "+s"(pws));

  const unsigned region = b * 64 + mem;
  const auto rk = __builtin_amdgcn_make_buffer_rsrc((void*)(pk + (size_t)region * REGION), 0, (int)REGION, 0x00020000);
  const unsigned voff = lane * 16 + ((wave & 1) << 13) + ((wave & 2) << 14);
  unsigned pos = mem;
  unsigned tq = (t0 + PACE_T0) << 8;
  unsigned dq = PACE_DQ1;
  u32x4 R[12][4];
  auto issue = [&](u32x4 (&dst)[4], const __amdgpu_buffer_rsrc_t& r) {
    const unsigned s = ((pos & 4u) << 10) | ((pos & 1u) << 14) | ((pos & 2u) << 15) | ((pos & 56u) << 14);
#pragma unroll
    for (int t = 0; t < 4; ++t) dst[t] = __builtin_amdgcn_raw_buffer_load_b128(r, voff + t * 1024, s, NT);
    pos += 1;
    tq += dq;
    PIN();
  };
  if (wave < 4) issue(R[0], rk);
  int role = wave;
  asm volatile("" : "+s"(role));

  const auto rw = __builtin_amdgcn_make_buffer_rsrc((void*)pws, 0, (int)WS_END, 0x00020000);
  bool ok = true;
  if (role == 4) {
    const auto rq = __builtin_amdgcn_make_buffer_rsrc((void*)(pwq + (size_t)sub * 128 * (NH * DH * 2) + h * (DH * 2)), 0, 128 * NH * DH * 2 - h * (DH * 2), 0x00020000);
    const auto rx = __builtin_amdgcn_make_buffer_rsrc((void*)px, 0, 2 * DM * 2, 0x00020000);
    const unsigned xo = (128 * sub + 4 * c + g) * 2;
    const unsigned x00 = __builtin_amdgcn_raw_buffer_load_b16(rx, xo, 0, NT), x01 = __builtin_amdgcn_raw_buffer_load_b16(rx, xo + DM * 2, 0, NT);
    const unsigned x10 = __builtin_amdgcn_raw_buffer_load_b16(rx, xo + 128, 0, NT), x11 = __builtin_amdgcn_raw_buffer_load_b16(rx, xo + 128 + DM * 2, 0, NT);
    u32x4 W[32];
    PIN();
    const unsigned vo = g * (NH * DH * 2) + c * 16;
#pragma unroll
    for (int t = 0; t < 32; ++t) W[t] = __builtin_amdgcn_raw_buffer_load_b128(rq, vo, t * 4 * (NH * DH * 2), NT);
    PIN();
    STAMP(7);
    const u32x2 prev = sload2(pws + WS_EH + ph * LINE);
    const unsigned tlo = prev[0] + 1u, thi = (prev[1] + (tlo == 0u ? 1u : 0u)) & 0xffu;
    if (lane < 4) *(volatile LAS unsigned*)(lds + L_ML + 4 * lane + 2) = 0u;
    const unsigned xa = x00 | (x01 << 16), xb = x10 | (x11 << 16);
    unsigned sel[4];
#pragma unroll
    for (int k = 0; k < 4; ++k) {
      const unsigned pick = (c & 8) ? 0x0302u : 0x0100u;
      sel[k] = ((c & 7) == 2 * k ? pick : 0x0c0cu) | (((c & 7) == 2 * k + 1 ? pick : 0x0c0cu) << 16);
    }
    f32x4 d0 = {0.f, 0.f, 0.f, 0.f}, d1 = {0.f, 0.f, 0.f, 0.f};
#define QM(T, X, D) { const unsigned xt = dppu<BCAST0 + ((T) & 15)>(X); \
      const u32x4 a = {__builtin_amdgcn_perm(xt, xt, sel[0]), __builtin_amdgcn_perm(xt, xt, sel[1]), __builtin_amdgcn_perm(xt, xt, sel[2]), __builtin_amdgcn_perm(xt, xt, sel[3])}; \
      D = __builtin_amdgcn_mfma_f32_16x16x32_bf16(__builtin_bit_cast(bf16x8, a), __builtin_bit_cast(bf16x8, W[T]), D, 0, 0, 0); }
#define QM4(T, X) QM(T, X, d0) QM(T + 1, X, d1) QM(T + 2, X, d0) QM(T + 3, X, d1) PIN();
    QM4(0, xa) QM4(4, xa) QM4(8, xa) QM4(12, xa) QM4(16, xb) QM4(20, xb) QM4(24, xb) QM4(28, xb)
#undef QM4
#undef QM
    d0 += d1;
    STAMP(8);
    const unsigned qo = WS_QX + ph * 8192 + sub * 2048 + (g >> 1) * 1024 + c * 64 + (g & 1) * 32;
    __builtin_amdgcn_raw_buffer_store_b128(granules(d0[0], d0[1], tlo, thi), rw, qo, 0, SC1);
    __builtin_amdgcn_raw_buffer_store_b128(granules(d0[2], d0[3], tlo, thi), rw, qo + 16, 0, SC1);
    STAMP(9);
    const unsigned ts = now();
    while (now() - ts < Q_DELAY) __builtin_amdgcn_s_sleep(1);
    u32x4 e[4];
    do {
#pragma unroll
      for (int k = 0; k < 4; ++k) e[k] = __builtin_amdgcn_raw_buffer_load_b128(rw, WS_QX + ph * 8192 + b * 1024 + lane * 16, k * 2048, SC1);
      ok = valid4(e, tlo, thi);
      if (!ok) __builtin_amdgcn_s_sleep(16);
    } while (!ok && now() - ts <= SPIN_LIMIT);
    STAMP(10);
    *(LAS unsigned*)(lds + L_Q + lane) = pack_bf16((val(e[0][1]) + val(e[1][1])) + (val(e[2][1]) + val(e[3][1])), (val(e[0][3]) + val(e[1][3])) + (val(e[2][3]) + val(e[3][3])));
    if (sub == 0 && lane == 0 && ok)
      __hip_atomic_store((GAS unsigned long long*)(pws + WS_EH + ph * LINE), ((unsigned long long)thi << 32) | tlo, __ATOMIC_RELAXED, __HIP_MEMORY_SCOPE_AGENT);
    wait_lgkm0();
  } else if (role == 5) {
    const u32x2 prev = sload2(pws + WS_EG + grp * LINE);
    const unsigned tlo = prev[0] + 1u, thi = (prev[1] + (tlo == 0u ? 1u : 0u)) & 0xffu;
    if (lane == 0) { *(LAS u32x4*)(lds + L_TAG) = u32x4{tlo, thi, PACE_DQ, 0u}; *(LAS u32x2*)(lds + L_SCL) = u32x2{0u, 0u}; }
    wait_lgkm0();
  }
  else if (role == 6) {
    const unsigned char* pc;
    asm volatile("s_getpc_b64 %0" : "=s"(pc));
    pc += (unsigned)(bid >> 3) * CODE_SLICE;
    unsigned d0, d1, d2, d3, d4, d5;
    asm volatile("s_load_dword %0, %6, 0x0\n\ts_load_dword %1, %6, 0x80\n\ts_load_dword %2, %6, 0x100\n\ts_load_dword %3, %6, 0x180\n\t"
                 "s_load_dword %4, %6, 0x200\n\ts_load_dword %5, %6, 0x280\n\ts_waitcnt lgkmcnt(0)"
                 : "=&s"(d0), "=&s"(d1), "=&s"(d2), "=&s"(d3), "=&s"(d4), "=&s"(d5) : "s"(pc) : "memory");
  }
  asm volatile("" : "+s"(role));

  if (role < 4) {
    __builtin_amdgcn_s_setprio(3);
    const auto rv = __builtin_amdgcn_make_buffer_rsrc((void*)(pv + (size_t)region * REGION), 0, (int)REGION, 0x00020000);
    auto gate = [&]() {
      PIN();
      wait_vm<4 * (WIN - 1)>();
      while ((int)((now() << 8) - tq) < 0) __builtin_amdgcn_s_sleep(1);
      PIN();
    };
    auto gate_ctl = [&](bool slow) {
      PIN();
      wait_vm<4 * (WIN - 1)>();
      const unsigned ctl = __builtin_amdgcn_readfirstlane(*(volatile LAS unsigned*)(lds + L_CTL));
      const int late = (int)((now() << 8) - tq);
      *(volatile LAS int*)(lds + L_LATE + wave * 64 + lane) = late > 0 ? late : 0;
      dq = (slow && ctl < PACE_DQ1) ? PACE_DQ1 : ctl;
      while ((int)((now() << 8) - tq) < 0) __builtin_amdgcn_s_sleep(1);
      PIN();
    };
    STAMP(6);
#pragma unroll
    for (int j = 1; j < 12; ++j) { gate(); if (j == 2) STAMP(7); if (j == 3) STAMP(8); if (j == 7) STAMP(9); issue(R[j], rk); }
    STAMP(1);
    __builtin_amdgcn_s_barrier();
    PIN();
    const u32x4 q = *(LAS const u32x4*)(lds + L_Q + 4 * c);
    STAMP(2);
    LAS float* sc = lds + L_SC + wave * 1024 + lane;
    float keep = 0.f, mx = -INFINITY;
    auto score = [&](const u32x4 (&K)[4], int J, int row) {
      float x[4];
#pragma unroll
      for (int t = 0; t < 4; ++t) {
        float d = dot2(K[t][0], q[0], 0.f);
        d = dot2(K[t][1], q[1], d);
        d = dot2(K[t][2], q[2], d);
        x[t] = dot2(K[t][3], q[3], d);
      }
      const float a0 = x[0] + dpp<XOR8>(x[0]), a1 = x[1] + dpp<XOR8>(x[1]), a2 = x[2] + dpp<XOR8>(x[2]), a3 = x[3] + dpp<XOR8>(x[3]);
      const float y0 = (c & 8) ? a2 : a0, y1 = (c & 8) ? a3 : a1;
      const float b0 = y0 + dpp<XOR7>(y0), b1 = y1 + dpp<XOR7>(y1);
      float s = (c & 4) ? b1 : b0;
      s += dpp<XOR2>(s);
      s += dpp<XOR1>(s);
      keep = ((c & 3) == J) ? s : keep;
      if (J == 3) { sc[row * 64] = keep; mx = fmaxf(mx, keep); }
      asm volatile("" : "+v"(keep), "+v"(mx));
    };
    score(R[0], 0, 0); score(R[1], 1, 0); score(R[2], 2, 0); score(R[3], 3, 0);
    gate_ctl(true); issue(R[0], rk);
    score(R[4], 0, 1); score(R[5], 1, 1); score(R[6], 2, 1); score(R[7], 3, 1);
    gate(); issue(R[1], rk);
    score(R[8], 0, 2); score(R[9], 1, 2); score(R[10], 2, 2);
    gate(); issue(R[2], rk);
    score(R[11], 3, 2);
    if (WIN == 2) score(R[0], 0, 3);
    gate(); issue(R[3], rk);
    score(R[3 - WIN], 3 - WIN, 3);
#pragma unroll 1
    for (int it = 0; it < 12; ++it) {
#pragma unroll
      for (int j = 0; j < 4; ++j) {
        if ((j & 1) == 0) gate_ctl(it < PACE_N1); else gate();
        issue(R[j], rk);
        score(R[(j + 4 - WIN) & 3], (j + 4 - WIN) & 3, j < WIN ? 3 + it : 4 + it);
      }
    }
#pragma unroll
    for (int j = 0; j < WIN; ++j) {
      gate();
      issue(R[j], rv);
      score(R[4 - WIN + j], 4 - WIN + j, 15);
    }
    mx = fmaxf(mx, dpp<XOR1>(mx)); mx = fmaxf(mx, dpp<XOR2>(mx)); mx = fmaxf(mx, dpp<XOR7>(mx)); mx = fmaxf(mx, dpp<XOR8>(mx));
    mx = xrow_max(mx);
    const float m2 = mx * C2;
    {
      float l = 0.f;
#pragma unroll
      for (int u = 0; u < 16; ++u) {
        const float p = __builtin_amdgcn_exp2f(fmaf(sc[u * 64], C2, -m2));
        sc[u * 64] = p;
        l += p;
      }
      l += dpp<XOR1>(l); l += dpp<XOR2>(l); l += dpp<XOR7>(l); l += dpp<XOR8>(l);
      l = swap16_add(l, l);
      l = swap32_add(l, l);
      if (lane == 0) {
        *(LAS f32x2*)(lds + L_ML + 4 * wave) = f32x2{m2, l};
        wait_lgkm0();
        *(volatile LAS unsigned*)(lds + L_ML + 4 * wave + 2) = 1u;
      }
    }
    STAMP(3);
    float acc[8];
#pragma unroll
    for (int k = 0; k < 8; ++k) acc[k] = 0.f;
#define PV(RR, J) do { axpy8<0 + J>(acc, p, RR[0]); axpy8<4 + J>(acc, p, RR[1]); axpy8<8 + J>(acc, p, RR[2]); axpy8<12 + J>(acc, p, RR[3]); \
    asm volatile("" : "+v"(acc[0]), "+v"(acc[1]), "+v"(acc[2]), "+v"(acc[3]), "+v"(acc[4]), "+v"(acc[5]), "+v"(acc[6]), "+v"(acc[7])); } while (0)
    float p = sc[0];
#pragma unroll 1
    for (int u = 0; u < 15; ++u) {
      const float pn = sc[(u + 1) * 64];
      gate_ctl(false); issue(R[(0 + WIN) & 3], rv); PV(R[0], 0);
      gate(); issue(R[(1 + WIN) & 3], rv); PV(R[1], 1);
      gate_ctl(false); issue(R[(2 + WIN) & 3], rv); PV(R[2], 2);
      gate(); issue(R[(3 + WIN) & 3], rv); PV(R[3], 3);
      p = pn;
    }
    gate(); issue(R[(0 + WIN) & 3], rv);
    if (WIN == 2) { PV(R[0], 0); gate(); issue(R[3], rv); }
    STAMP(4);
    PIN();
    if (WIN == 3) PV(R[0], 0);
    PV(R[1], 1);
    PV(R[2], 2);
    PV(R[3], 3);
#undef PV
    *(LAS f32x2*)(lds + L_OP + wave * 128 + 2 * lane) = fold_rows(acc);
    wait_lgkm0();
    STAMP(5);
    __builtin_amdgcn_s_barrier();
    STAMP_FLUSH();
    return;
  }

  const int hw = wave - 4;
  STAMP(1);
  __builtin_amdgcn_s_barrier();
  PIN();
  const u32x2 tg2 = *(LAS const u32x2*)(lds + L_TAG);
  const unsigned tlo = __builtin_amdgcn_readfirstlane(tg2[0]), thi = __builtin_amdgcn_readfirstlane(tg2[1]);
  const unsigned yoff = WS_Y + (grp * 64 + mem) * 4096 + hw * 1024 + c * 64 + g * 16;
  const unsigned gath = WS_Y + (grp * 64 + (lane >> 2)) * 4096 + mem * 64 + (lane & 3) * 16;
  constexpr unsigned GSTR = 16 * 4096;
  auto stats = [&](float (&f)[4], float& m, float& l) {
    const unsigned ts = now();
    for (;;) {
      const unsigned fl = *(volatile LAS unsigned*)(lds + L_ML + 4 * (lane & 3) + 2);
      if (__all(fl == 1u) || now() - ts > SPIN_LIMIT) break;
      __builtin_amdgcn_s_sleep(16);
    }
    asm volatile("" ::: "memory");
    float mw[4], lw[4];
#pragma unroll
    for (int w = 0; w < 4; ++w) { const f32x2 ml = *(volatile LAS f32x2*)(lds + L_ML + 4 * w); mw[w] = ml[0]; lw[w] = ml[1]; }
    m = round24(fmaxf(fmaxf(mw[0], mw[1]), fmaxf(mw[2], mw[3])));
    l = 0.f;
#pragma unroll
    for (int w = 0; w < 4; ++w) { f[w] = __builtin_amdgcn_exp2f(mw[w] - m); l = fmaf(f[w], lw[w], l); }
  };
  float f[4];
  {
    const auto ro = __builtin_amdgcn_make_buffer_rsrc((void*)pwo, 0, DM * NH * DH * 2, 0x00020000);
    const unsigned wvo = (unsigned)(h * DH + 2 * g) * (DM * 2) + hw * 256 + c * 16;
    unsigned tw = t0 + WO_T0 + (unsigned)(((h * 2 + prob) * WO_GAP) >> 6);
    unsigned tr = ((t0 + AD_T0) >> AD_LGP) << AD_LGP, tc = tr;
    float c_int = 0.f;
#pragma unroll 1
    for (int t = 0; t < 32; ++t) {
      while ((int)(now() - tw) < 0) {
        if (ADAPT && hw == 1) {
          if ((bid & 0x68) == 0 && (int)(now() - tr) >= 0) {
            float a = (float)*(volatile LAS int*)(lds + L_LATE + (lane & 3) * 64);
            a += dpp<XOR1>(a); a += dpp<XOR2>(a);
            const u32x4 gr = granules(a * (0.25f / 256.f), 0.f, tlo, thi);
            if (lane == 0) __builtin_amdgcn_raw_buffer_store_b64(u32x2{gr[0], gr[1]}, rw, WS_CTL + (((tr >> AD_LGP) & 63u) * 4 + grp) * LINE + (bid & 7) * 8, 0, SC1);
            tr += 1u << AD_LGP;
          }
          if ((int)(now() - tc) >= AD_D) {
            const unsigned slot = WS_CTL + (((tc >> AD_LGP) & 63u) * 4 + grp) * LINE;
            const u32x16 e = sload16(pws + slot);
            float n = 0.f, s1 = 0.f, hi = 0.f, lo = 0.f;
#pragma unroll
            for (int k = 0; k < 8; ++k) {
              const bool v0 = e[2 * k] == tlo && (e[2 * k + 1] & 0xffu) == thi;
              const float l0 = v0 ? val(e[2 * k + 1]) : 0.f;
              n += v0 ? 1.f : 0.f;
              s1 += l0;
              lo = fmaxf(lo, fminf(hi, l0));
              hi = fmaxf(hi, l0);
            }
            const float err = AD_MODE == 0 ? s1 / fmaxf(n, 1.f) : AD_MODE == 1 ? hi : 0.5f * (hi + lo);
            const float pterm = fmaxf(err - AD_CALM, 0.f) * (1.f / AD_TP);
            if (n >= 5.f) {
              c_int = err < AD_CALM ? fmaxf(c_int - AD_LEAK, 0.f) : fminf(c_int + AD_KI * pterm, AD_MAX);
              if (lane == 0) *(volatile LAS unsigned*)(lds + L_CTL) = (unsigned)((float)PACE_DQ * (1.f + fminf(c_int + pterm, AD_MAX)));
            }
            tc += 1u << AD_LGP;
          }
        }
        __builtin_amdgcn_s_sleep(4);
      }
      tw += WO_GAP;
      __builtin_amdgcn_raw_ptr_buffer_load_lds(ro, (LAS void*)(lds + L_WO + (hw * 32 + t) * 256), 16, wvo, ((t >> 1) * 8 + (t & 1)) * (DM * 2), 0, NT);
      if (t == 8 && hw == 2) {
        sload2(pv + (size_t)region * REGION);
        sload2(pws + WS_Y + grp * 64 * 4096 + mem * 4096);
        sload2(pout);
      }
      if (t == 16 && hw == 0) {
        float m, l;
        stats(f, m, l);
        if (lane == 0) __builtin_amdgcn_raw_buffer_store_b128(granules(m, l, tlo, thi), rw, WS_ML + bid * LINE, 0, SC1);
      }
      if (t == 25 && hw == 0) {
        const unsigned ts = now();
        u32x4 e;
        for (;;) {
          e = __builtin_amdgcn_raw_buffer_load_b128(rw, WS_ML + (bid ^ ((lane & 1) << 3)) * LINE, 0, SC1);
          if (__all(valid(e, tlo, thi))) break;
          if (now() - ts > SPIN_LIMIT) { ok = false; break; }
          __builtin_amdgcn_s_sleep(64);
        }
        const float m0 = val(e[1]), l0 = val(e[3]);
        const float m1 = dpp<XOR1>(m0), l1 = dpp<XOR1>(l0);
        const float M = fmaxf(m0, m1);
        const float e0 = __builtin_amdgcn_exp2f(m0 - M), e1 = __builtin_amdgcn_exp2f(m1 - M);
        const float wv = ok ? e0 / fmaf(e0, l0, e1 * l1) : __builtin_nanf("");
        if (lane == 0) {
          *(volatile LAS float*)(lds + L_SCL) = wv;
          wait_lgkm0();
          *(volatile LAS unsigned*)(lds + L_SCL + 1) = 1u;
        }
      }
    }
  }
  STAMP(2);
  {
    float m, l;
    stats(f, m, l);
    const unsigned ts = now();
    while (*(volatile LAS unsigned*)(lds + L_SCL + 1) != 1u && now() - ts <= SPIN_LIMIT) __builtin_amdgcn_s_sleep(4);
    const float scl = *(volatile LAS float*)(lds + L_SCL);
#pragma unroll
    for (int w = 0; w < 4; ++w) f[w] *= scl;
  }
  asm volatile("s_waitcnt vmcnt(0)" ::: "memory");
  u32x4 W[32];
#pragma unroll
  for (int u = 0; u < 16; ++u) {
    const u32x4 r0 = *(LAS const u32x4*)(lds + L_WO + (hw * 32 + 2 * u) * 256 + lane * 4), r1 = *(LAS const u32x4*)(lds + L_WO + (hw * 32 + 2 * u + 1) * 256 + lane * 4);
#pragma unroll
    for (int k = 0; k < 2; ++k) {
      W[2 * u][2 * k] = __builtin_amdgcn_perm(r1[k], r0[k], 0x05040100u);
      W[2 * u][2 * k + 1] = __builtin_amdgcn_perm(r1[k], r0[k], 0x07060302u);
      W[2 * u + 1][2 * k] = __builtin_amdgcn_perm(r1[k + 2], r0[k + 2], 0x05040100u);
      W[2 * u + 1][2 * k + 1] = __builtin_amdgcn_perm(r1[k + 2], r0[k + 2], 0x07060302u);
    }
  }
#pragma unroll
  for (int t = 0; t < 32; ++t) asm volatile("" : "+v"(W[t]));
  STAMP(3);
  __builtin_amdgcn_s_setprio(3);
  __builtin_amdgcn_s_barrier();
  PIN();
  STAMP(4);
  {
    f32x2 o = f[0] * *(LAS const f32x2*)(lds + L_OP + 2 * lane);
#pragma unroll
    for (int w = 1; w < 4; ++w) o += f[w] * *(LAS const f32x2*)(lds + L_OP + w * 128 + 2 * lane);
    const unsigned op = pack_bf16(o[0], o[1]);
    float y[8];
#pragma unroll
    for (int k = 0; k < 8; ++k) y[k] = 0.f;
#define O1(U) { const unsigned ob = dppu<BCAST0 + (U)>(op); _Pragma("unroll") for (int k = 0; k < 4; ++k) { y[k] = dot2(W[2 * (U)][k], ob, y[k]); y[4 + k] = dot2(W[2 * (U) + 1][k], ob, y[4 + k]); } }
    O1(0) O1(1) O1(2) O1(3) O1(4) O1(5) O1(6) O1(7) O1(8) O1(9) O1(10) O1(11) O1(12) O1(13) O1(14) O1(15)
#undef O1
    const f32x2 yy = fold_rows(y);
    __builtin_amdgcn_raw_buffer_store_b128(granules(yy[0], yy[1], tlo, thi), rw, yoff, 0, SC1);
  }
  STAMP(5);
  if (hw != 0) { STAMP_FLUSH(); return; }
  {
    u32x4 e[6][4];
    float s0 = 0.f, s1 = 0.f;
    auto fetch = [&](u32x4 (&d)[4]) {
#pragma unroll
      for (int k = 0; k < 4; ++k) d[k] = __builtin_amdgcn_raw_buffer_load_b128(rw, gath, k * GSTR, SC1);
      PIN();
    };
    auto take = [&](const u32x4 (&d)[4]) {
      s0 = (val(d[0][1]) + val(d[1][1])) + (val(d[2][1]) + val(d[3][1]));
      s1 = (val(d[0][3]) + val(d[1][3])) + (val(d[2][3]) + val(d[3][3]));
      return valid4(d, tlo, thi);
    };
    if (POLL_T0) __builtin_amdgcn_s_sleep(POLL_T0);
    fetch(e[0]); __builtin_amdgcn_s_sleep(POLL_GAP);
    fetch(e[1]); __builtin_amdgcn_s_sleep(POLL_GAP);
    fetch(e[2]);
    bool all = take(e[0]);
    if (!all) { fetch(e[3]); all = take(e[1]);
      if (!all) { fetch(e[4]); all = take(e[2]);
        if (!all) { fetch(e[5]); all = take(e[3]);
          if (!all) { all = take(e[4]);
            if (!all) {
              const unsigned ts = now();
              bool v[4];
#pragma unroll
              for (int k = 0; k < 4; ++k) v[k] = valid(e[5][k], tlo, thi);
              while (!(all = take(e[5])) && now() - ts <= SPIN_LIMIT) {
                __builtin_amdgcn_s_sleep(4);
#pragma unroll
                for (int k = 0; k < 4; ++k) e[4][k] = __builtin_amdgcn_raw_buffer_load_b128(rw, v[k] ? WS_END : gath + k * GSTR, 0, SC1);
#pragma unroll
                for (int k = 0; k < 4; ++k) { if (!v[k]) e[5][k] = e[4][k]; v[k] = valid(e[5][k], tlo, thi); }
              }
            } } } } }
    ok = ok && all;
    s0 += dpp<XOR8>(s0); s1 += dpp<XOR8>(s1);
    s0 += dpp<ROR4>(s0); s1 += dpp<ROR4>(s1);
    s0 = swap16_add(s0, s0); s1 = swap16_add(s1, s1);
    s0 = swap32_add(s0, s0); s1 = swap32_add(s1, s1);
    if (!ok) { s0 = __builtin_nanf(""); s1 = s0; }
    if (lane < 4) ((GAS unsigned*)pout)[(b * 2 * DM + prob * DM + mem * 8) / 2 + lane] = pack_bf16(s0, s1);
    STAMP(6);
    if (mem == 0 && lane == 0 && all)
      __hip_atomic_store((GAS unsigned long long*)(pws + WS_EG + grp * LINE), ((unsigned long long)thi << 32) | tlo, __ATOMIC_RELAXED, __HIP_MEMORY_SCOPE_AGENT);
    PIN();
#pragma unroll
    for (int i = 0; i < 6; ++i) asm volatile("" :: "v"(e[i][0]), "v"(e[i][1]), "v"(e[i][2]), "v"(e[i][3]));
  }
  STAMP(11);
  STAMP_FLUSH();
}

extern "C" void kernel_launch(void* const* d_in, const int* in_sizes, int n_in, void* d_out, int out_size, void* d_ws,
                              size_t ws_size, hipStream_t stream) {
  Params P;
  for (int p = 0; p < 2; ++p) {
    P.x[p] = (const unsigned char*)d_in[5 * p + 0];
    P.k[p] = (const unsigned char*)d_in[5 * p + 1];
    P.v[p] = (const unsigned char*)d_in[5 * p + 2];
    P.wq[p] = (const unsigned char*)d_in[5 * p + 3];
    P.wo[p] = (const unsigned char*)d_in[5 * p + 4];
  }
  P.out = (unsigned char*)d_out;
  P.ws = (unsigned char*)d_ws;
  mha_decode_kernel<<<NWG, NTHR, 0, stream>>>(P);
}
```

```cpp
#include <hip/hip_runtime.h>
#include <hip/hip_bf16.h>
#include <stdint.h>

typedef unsigned u32x2 __attribute__((ext_vector_type(2)));
typedef unsigned u32x4 __attribute__((ext_vector_type(4)));
typedef unsigned u32x16 __attribute__((ext_vector_type(16)));
typedef float f32x2 __attribute__((ext_vector_type(2)));
typedef float f32x4 __attribute__((ext_vector_type(4)));
typedef __bf16 bf16x2 __attribute__((ext_vector_type(2)));
typedef __bf16 bf16x8 __attribute__((ext_vector_type(8)));
#define LAS __attribute__((address_space(3)))
#define GAS __attribute__((address_space(1)))

#define PACE_DQ 13700
#define PACE_DQ1 14900
#define PACE_N1 1
#define PACE_T0 200
#define ROT (2 * h + (qd >> 1) + 4 * (qd & 1))
#define WIN 3
#define Q_DELAY 200
#define WO_T0 1700
#define WO_GAP0 52
#define WO_GAP1 80
#define WO_AUX 2
#define PARK_AUX 16
#define PARK_N 2
#define WO_PRE 180
#define WS_AT_END 1

#define POLL_T0 18
#define POLL_GAP 6
#define CODE_SLICE 768
#define ADAPT 1
#define AD_LGP 8
#define AD_D 180
#define AD_T0 1100
#define AD_TP 1200.f
#define AD_STEP 0.006f
#define AD_RISE 10.0f
#define AD_TI 1500
#define AD_LEAK 0.0015f
#define AD_CALM 135.0f
#define AD_SIG 1
#define AD_MAX 0.2f

constexpr int NH = 32, DH = 128, DM = 512;
constexpr int NWG = 256, NTHR = 512;
constexpr unsigned REGION = 1u << 19;
constexpr int NT = 2, SC1 = 16;
constexpr float C2 = 0.08838834764831845f * 1.4426950408889634f;

constexpr unsigned LINE = 256;
constexpr unsigned WS_EG = 0, WS_EH = WS_EG + 2 * LINE, WS_ML = WS_EH + 32 * LINE, WS_CTL = WS_ML + 2 * NWG * LINE, WS_QX = WS_CTL + 128 * LINE,
                   WS_Y = WS_QX + 32 * 16384, WS_END = WS_Y + NWG * 4096;
constexpr int L_SC = 0;
constexpr int L_OP = L_SC + 2048;
constexpr int L_Q = L_OP + 512;
constexpr int L_ML = L_Q + 128;
constexpr int L_TAG = L_ML + 16;
constexpr int L_CTL = L_TAG + 2;
constexpr int L_SCL = L_TAG + 4;
constexpr int L_LATE = L_TAG + 8;
constexpr int L_WO = L_LATE + 256;
constexpr int L_END = L_WO + 32768;

struct Params {
  const unsigned char* x[2];
  const unsigned char* k[2];
  const unsigned char* v[2];
  const unsigned char* wq[2];
  const unsigned char* wo[2];
  unsigned char* out;
  unsigned char* ws;
};

__device__ __forceinline__ float dot2(unsigned a, unsigned b, float acc) {
  return __builtin_amdgcn_fdot2_f32_bf16(__builtin_bit_cast(bf16x2, a), __builtin_bit_cast(bf16x2, b), acc, false);
}
template <int CTRL> __device__ __forceinline__ unsigned dppu(unsigned x) { return (unsigned)__builtin_amdgcn_mov_dpp((int)x, CTRL, 0xf, 0xf, true); }
template <int CTRL> __device__ __forceinline__ float dpp(float x) { return __builtin_bit_cast(float, dppu<CTRL>(__builtin_bit_cast(unsigned, x))); }
constexpr int XOR1 = 0xB1, XOR2 = 0x4E, XOR7 = 0x141, XOR8 = 0x128, ROR4 = 0x124, BCAST0 = 0x150;
__device__ __forceinline__ float swap16_add(float a, float b) {
  auto s = __builtin_amdgcn_permlane16_swap(__float_as_uint(a), __float_as_uint(b), false, false);
  return __uint_as_float(s[0]) + __uint_as_float(s[1]);
}
__device__ __forceinline__ float swap32_add(float a, float b) {
  auto s = __builtin_amdgcn_permlane32_swap(__float_as_uint(a), __float_as_uint(b), false, false);
  return __uint_as_float(s[0]) + __uint_as_float(s[1]);
}
__device__ __forceinline__ float xrow_max(float x) {
  auto s = __builtin_amdgcn_permlane16_swap(__float_as_uint(x), __float_as_uint(x), false, false);
  x = fmaxf(__uint_as_float(s[0]), __uint_as_float(s[1]));
  auto t = __builtin_amdgcn_permlane32_swap(__float_as_uint(x), __float_as_uint(x), false, false);
  return fmaxf(__uint_as_float(t[0]), __uint_as_float(t[1]));
}
__device__ __forceinline__ f32x2 fold_rows(const float (&a)[8]) {
  const float z0 = swap32_add(a[0], a[4]), z1 = swap32_add(a[1], a[5]), z2 = swap32_add(a[2], a[6]), z3 = swap32_add(a[3], a[7]);
  return f32x2{swap16_add(z0, z2), swap16_add(z1, z3)};
}
__device__ __forceinline__ unsigned pack_bf16(float lo, float hi) {
  bf16x2 r;
  r[0] = (__bf16)lo;
  r[1] = (__bf16)hi;
  return __builtin_bit_cast(unsigned, r);
}
template <int T> __device__ __forceinline__ u32x4 diag_of(unsigned w, const u32x4& diag) {
  return u32x4{dppu<BCAST0 + T>(w) & diag[0], dppu<BCAST0 + T>(w) & diag[1], dppu<BCAST0 + T>(w) & diag[2], dppu<BCAST0 + T>(w) & diag[3]};
}
__device__ __forceinline__ f32x4 rows_mac(f32x4 acc, const u32x4& a, const u32x4& v) {
  return __builtin_amdgcn_mfma_f32_16x16x32_bf16(__builtin_bit_cast(bf16x8, a), __builtin_bit_cast(bf16x8, v), acc, 0, 0, 0);
}
__device__ __forceinline__ u32x4 diag_mask(int c) {
  const unsigned half = (c & 1) ? 0xffff0000u : 0x0000ffffu;
  return u32x4{(c >> 1) == 0 ? half : 0u, (c >> 1) == 1 ? half : 0u, (c >> 1) == 2 ? half : 0u, (c >> 1) == 3 ? half : 0u};
}
__device__ __forceinline__ float round24(float v) { return __uint_as_float((__float_as_uint(v) + 0x80u) & 0xffffff00u); }
__device__ __forceinline__ u32x4 granules(float v0, float v1, unsigned tlo, unsigned thi) {
  return u32x4{tlo, __float_as_uint(round24(v0)) | thi, tlo, __float_as_uint(round24(v1)) | thi};
}
__device__ __forceinline__ bool valid(const u32x4& e, unsigned tlo, unsigned thi) {
  return (((e[0] ^ tlo) | (e[2] ^ tlo)) | (((e[1] ^ thi) | (e[3] ^ thi)) & 0xffu)) == 0u;
}
__device__ __forceinline__ bool valid4(const u32x4 (&e)[4], unsigned tlo, unsigned thi) {
  return __all((int)valid(e[0], tlo, thi) & (int)valid(e[1], tlo, thi) & (int)valid(e[2], tlo, thi) & (int)valid(e[3], tlo, thi));
}
__device__ __forceinline__ float val(unsigned u) { return __uint_as_float(u & 0xffffff00u); }
__device__ __forceinline__ u32x2 pair_granule(float v0, float v1, unsigned tag24) {
  const unsigned r0 = (__float_as_uint(v0) + 0x800u) & 0xfffff000u, r1 = __float_as_uint(v1) + 0x800u;
  return u32x2{((r1 << 12) & 0xff000000u) | tag24, r0 | (r1 >> 20)};
}
__device__ __forceinline__ float pair_first(unsigned hi) { return __uint_as_float(hi & 0xfffff000u); }
__device__ __forceinline__ float pair_second(unsigned lo, unsigned hi) { return __uint_as_float((hi << 20) | ((lo >> 12) & 0xff000u)); }
template <int N> __device__ __forceinline__ void wait_vm() { __builtin_amdgcn_s_waitcnt((N & 15) | ((N >> 4) << 14) | (7 << 4) | (15 << 8)); }
__device__ __forceinline__ void wait_lgkm0() { __builtin_amdgcn_s_waitcnt(0xC07F); }
__device__ __forceinline__ unsigned now() { return (unsigned)__builtin_amdgcn_s_memrealtime(); }
__device__ __forceinline__ u32x2 sload2(const unsigned char* p) {
  u32x2 e;
  asm volatile("s_load_dwordx2 %0, %1, 0x0\n\ts_waitcnt lgkmcnt(0)" : "=&s"(e) : "s"(p) : "memory");
  return e;
}
__device__ __forceinline__ u32x16 sload16(const unsigned char* p) {
  u32x16 e;
  asm volatile("s_load_dwordx16 %0, %1, 0x0\n\ts_waitcnt lgkmcnt(0)" : "=&s"(e) : "s"(p) : "memory");
  return e;
}
#define PIN() __builtin_amdgcn_sched_barrier(0)
constexpr unsigned SPIN_LIMIT = 5000000u;

#define STAMP(i) do { } while (0)
#define STAMP_FLUSH() do { } while (0)

__global__ void __launch_bounds__(NTHR, 2) mha_decode_kernel(Params P) {
  __shared__ __attribute__((aligned(16))) float lds_raw[L_END];
  LAS float* lds = (LAS float*)lds_raw;
  STAMP(0);
  const unsigned t0 = now();
  PIN();
  const int bid = blockIdx.x;
  const int h = (bid & 7) * 4 + (bid >> 6), sub = (bid >> 3) & 7;
  const int b = sub >> 2, qd = sub & 3;
  const int lane = threadIdx.x & 63;
  const int wave = __builtin_amdgcn_readfirstlane(threadIdx.x >> 6);
  const int g = lane >> 4, c = lane & 15;
  const unsigned mem = 4 * h + qd;

  const size_t rofs = (size_t)(b * 128 + mem) * REGION;
  const unsigned char *px = b ? P.x[1] : P.x[0], *pwq = b ? P.wq[1] : P.wq[0];
  const unsigned char *pk0 = P.k[0] + rofs, *pk1 = P.k[1] + rofs, *pv0 = P.v[0] + rofs, *pv1 = P.v[1] + rofs, *pwo0 = P.wo[0], *pwo1 = P.wo[1];
  unsigned char *pout = P.out, *pws = P.ws;
  asm volatile("" : "+s"(px), "+s"(pwq), "+s"(pk0), "+s"(pk1), "+s"(pv0), "+s"(pv1), "+s"(pwo0), "+s"(pwo1), "+s"(pout), "+s"(pws));

  const unsigned voff = lane * 16 + ((wave & 1) << 13) + ((wave & 2) << 14);
  unsigned pos = ROT;
  unsigned tq = (t0 + PACE_T0) << 8;
  unsigned dq = PACE_DQ1;
  u32x4 R[12][4];
  auto issue = [&](u32x4 (&dst)[4], const __amdgpu_buffer_rsrc_t& r) {
    const unsigned s = ((pos & 4u) << 10) | ((pos & 1u) << 14) | ((pos & 2u) << 15) | ((pos & 24u) << 14);
#pragma unroll
    for (int t = 0; t < 4; ++t) dst[t] = __builtin_amdgcn_raw_buffer_load_b128(r, voff + t * 1024, s, NT);
    pos += 1;
    tq += dq;
    PIN();
  };
  auto rk = __builtin_amdgcn_make_buffer_rsrc((void*)pk0, 0, (int)REGION, 0x00020000);
  if (wave < 4) issue(R[0], rk);
  int role = wave;
  asm volatile("" : "+s"(role));

  const auto rw = __builtin_amdgcn_make_buffer_rsrc((void*)pws, 0, (int)WS_END, 0x00020000);
  bool ok = true;
  unsigned qlo = 0, qhi = 0;
  auto gather_q = [&](int p) {
    const unsigned ts = now();
    u32x4 e[4];
    bool all;
    do {
#pragma unroll
      for (int k = 0; k < 4; ++k) e[k] = __builtin_amdgcn_raw_buffer_load_b128(rw, WS_QX + h * 16384 + p * 8192 + b * 1024 + lane * 16, k * 2048, SC1);
      all = valid4(e, qlo, qhi);
      if (!all) __builtin_amdgcn_s_sleep(16);
    } while (!all && now() - ts <= SPIN_LIMIT);
    ok = ok && all;
    *(LAS unsigned*)(lds + L_Q + p * 64 + 4 * c + 2 * (g & 1) + (g >> 1)) =
        pack_bf16((val(e[0][1]) + val(e[1][1])) + (val(e[2][1]) + val(e[3][1])), (val(e[0][3]) + val(e[1][3])) + (val(e[2][3]) + val(e[3][3])));
  };
  auto park_wo = [&](const unsigned char* pw) {
    const auto r = __builtin_amdgcn_make_buffer_rsrc((void*)(pw + h * 131072 + sub * 16384), 0, 16384, 0x00020000);
    const unsigned d0 = __builtin_amdgcn_raw_buffer_load_b32(r, lane * 128, 0, NT);
    PIN();
    const unsigned e0 = __builtin_amdgcn_raw_buffer_load_b32(r, lane * 128 + 64, 0, PARK_AUX);
    PIN();
    const unsigned d1 = __builtin_amdgcn_raw_buffer_load_b32(r, lane * 128, 8192, NT);
    PIN();
    const unsigned e1 = __builtin_amdgcn_raw_buffer_load_b32(r, lane * 128 + 64, 8192, PARK_AUX);
    PIN();
    asm volatile("" :: "v"(d0), "v"(e0), "v"(d1), "v"(e1));
  };
  if (role == 4) {
    const auto rq = __builtin_amdgcn_make_buffer_rsrc((void*)(pwq + (size_t)qd * 128 * (NH * DH * 2) + h * (DH * 2)), 0, 128 * NH * DH * 2 - h * (DH * 2), 0x00020000);
    const auto rx = __builtin_amdgcn_make_buffer_rsrc((void*)px, 0, 2 * DM * 2, 0x00020000);
    const unsigned xo = (128 * qd + 4 * c + g) * 2;
    const unsigned x00 = __builtin_amdgcn_raw_buffer_load_b16(rx, xo, 0, NT), x01 = __builtin_amdgcn_raw_buffer_load_b16(rx, xo + DM * 2, 0, NT);
    const unsigned x10 = __builtin_amdgcn_raw_buffer_load_b16(rx, xo + 128, 0, NT), x11 = __builtin_amdgcn_raw_buffer_load_b16(rx, xo + 128 + DM * 2, 0, NT);
    u32x4 W[32];
    PIN();
    const unsigned vo = g * (NH * DH * 2) + c * 16;
#pragma unroll
    for (int t = 0; t < 32; ++t) W[t] = __builtin_amdgcn_raw_buffer_load_b128(rq, vo, t * 4 * (NH * DH * 2), NT);
    PIN();
    STAMP(7);
    const u32x2 prev = sload2(pws + WS_EH + h * LINE);
    qlo = prev[0] + 1u;
    qhi = (prev[1] + (qlo == 0u ? 1u : 0u)) & 0xffu;
    if (lane < 4) *(volatile LAS unsigned*)(lds + L_ML + 4 * lane + 2) = 0u;
    const unsigned xa = x00 | (x01 << 16), xb = x10 | (x11 << 16);
    unsigned sel[4];
#pragma unroll
    for (int k = 0; k < 4; ++k) {
      const unsigned pick = (c & 8) ? 0x0302u : 0x0100u;
      sel[k] = ((c & 7) == 2 * k ? pick : 0x0c0cu) | (((c & 7) == 2 * k + 1 ? pick : 0x0c0cu) << 16);
    }
    f32x4 d0 = {0.f, 0.f, 0.f, 0.f}, d1 = {0.f, 0.f, 0.f, 0.f};
#define QM(T, X, D) { const unsigned xt = dppu<BCAST0 + ((T) & 15)>(X); \
      const u32x4 a = {__builtin_amdgcn_perm(xt, xt, sel[0]), __builtin_amdgcn_perm(xt, xt, sel[1]), __builtin_amdgcn_perm(xt, xt, sel[2]), __builtin_amdgcn_perm(xt, xt, sel[3])}; \
      D = rows_mac(D, a, W[T]); }
#define QM4(T, X) QM(T, X, d0) QM(T + 1, X, d1) QM(T + 2, X, d0) QM(T + 3, X, d1) PIN();
    QM4(0, xa) QM4(4, xa) QM4(8, xa) QM4(12, xa) QM4(16, xb) QM4(20, xb) QM4(24, xb) QM4(28, xb)
#undef QM4
#undef QM
    d0 += d1;
    STAMP(8);
    const unsigned qo = WS_QX + h * 16384 + sub * 2048 + (g >> 1) * 1024 + (lane & 31) * 16;
    __builtin_amdgcn_raw_buffer_store_b128(granules(d0[0], d0[1], qlo, qhi), rw, qo, 0, SC1);
    __builtin_amdgcn_raw_buffer_store_b128(granules(d0[2], d0[3], qlo, qhi), rw, qo + 512, 0, SC1);
    STAMP(9);
    const unsigned ts = now();
    while (now() - ts < Q_DELAY) __builtin_amdgcn_s_sleep(1);
    gather_q(0);
    STAMP(10);
    wait_lgkm0();
  } else if (role == 5) {
    const u32x2 prev = sload2(pws + WS_EG + b * LINE);
    const unsigned tlo = prev[0] + 1u, thi = (prev[1] + (tlo == 0u ? 1u : 0u)) & 0xffu;
    if (lane == 0) { *(LAS u32x4*)(lds + L_TAG) = u32x4{tlo, thi, PACE_DQ, 0u}; *(LAS u32x4*)(lds + L_SCL) = u32x4{0u, 0u, 0u, 0u}; }
    wait_lgkm0();
  }
  else if (role == 7) {
    while (now() - t0 < WO_PRE) __builtin_amdgcn_s_sleep(1);
    if (PARK_N & 2) park_wo(pwo0);
    if (PARK_N != 3) park_wo(pwo1);
  }
  else if (role == 6) {
    const unsigned char* pc;
    asm volatile("s_getpc_b64 %0" : "=s"(pc));
    pc += (unsigned)(bid >> 3) * CODE_SLICE;
    unsigned d0, d1, d2, d3, d4, d5;
    asm volatile("s_load_dword %0, %6, 0x0\n\ts_load_dword %1, %6, 0x80\n\ts_load_dword %2, %6, 0x100\n\ts_load_dword %3, %6, 0x180\n\t"
                 "s_load_dword %4, %6, 0x200\n\ts_load_dword %5, %6, 0x280\n\ts_waitcnt lgkmcnt(0)"
                 : "=&s"(d0), "=&s"(d1), "=&s"(d2), "=&s"(d3), "=&s"(d4), "=&s"(d5) : "s"(pc) : "memory");
  }
  asm volatile("" : "+s"(role));

  if (role < 4) {
    __builtin_amdgcn_s_setprio(3);
    auto gate = [&]() {
      PIN();
      wait_vm<4 * (WIN - 1)>();
      while ((int)((now() << 8) - tq) < 0) __builtin_amdgcn_s_sleep(1);
      PIN();
    };
    auto gate_ctl = [&](bool slow, bool probe) {
      PIN();
      if (probe) wait_vm<4 * AD_SIG>(); else wait_vm<4 * (WIN - 1)>();
      const unsigned ctl = __builtin_amdgcn_readfirstlane(*(volatile LAS unsigned*)(lds + L_CTL));
      if (probe) *(volatile LAS int*)(lds + L_LATE + wave * 64 + lane) = (int)((now() << 8) - tq + (AD_SIG + 1) * dq);
      dq = (slow && ctl < PACE_DQ1) ? PACE_DQ1 : ctl;
      while ((int)((now() << 8) - tq) < 0) __builtin_amdgcn_s_sleep(1);
      PIN();
    };
    STAMP(6);
#pragma unroll
    for (int j = 1; j < 12; ++j) { gate(); if (j == 2) STAMP(7); if (j == 3) STAMP(8); if (j == 7) STAMP(9); issue(R[j], rk); }
    STAMP(1);
    __builtin_amdgcn_s_barrier();
    PIN();
    u32x4 q = *(LAS const u32x4*)(lds + L_Q + 4 * c);
    STAMP(2);
    LAS float* sc = lds + L_SC + wave * 512 + lane;
    float keep = 0.f, mx = -INFINITY;
    auto score = [&](const u32x4 (&K)[4], int J, int row) {
      float x[4];
#pragma unroll
      for (int t = 0; t < 4; ++t) {
        float d = dot2(K[t][0], q[0], 0.f);
        d = dot2(K[t][1], q[1], d);
        d = dot2(K[t][2], q[2], d);
        x[t] = dot2(K[t][3], q[3], d);
      }
      const float a0 = x[0] + dpp<XOR8>(x[0]), a1 = x[1] + dpp<XOR8>(x[1]), a2 = x[2] + dpp<XOR8>(x[2]), a3 = x[3] + dpp<XOR8>(x[3]);
      const float y0 = (c & 8) ? a2 : a0, y1 = (c & 8) ? a3 : a1;
      const float b0 = y0 + dpp<XOR7>(y0), b1 = y1 + dpp<XOR7>(y1);
      float s = (c & 4) ? b1 : b0;
      s += dpp<XOR2>(s);
      s += dpp<XOR1>(s);
      keep = ((c & 3) == J) ? s : keep;
      if (J == 3) { sc[row * 64] = keep; mx = fmaxf(mx, keep); }
      asm volatile("" : "+v"(keep), "+v"(mx));
    };
    score(R[0], 0, 0); score(R[1], 1, 0); score(R[2], 2, 0); score(R[3], 3, 0);
    gate_ctl(true, true); issue(R[0], rk);
    score(R[4], 0, 1); score(R[5], 1, 1); score(R[6], 2, 1); score(R[7], 3, 1);
    gate(); issue(R[1], rk);
    score(R[8], 0, 2); score(R[9], 1, 2); score(R[10], 2, 2);
    gate(); issue(R[2], rk);
    score(R[11], 3, 2);
    if (WIN == 2) score(R[0], 0, 3);
    gate(); issue(R[3], rk);
    score(R[3 - WIN], 3 - WIN, 3);
    const u32x4 diag = diag_mask(c);
    int it = 4, slow_end = 4 + PACE_N1;
#pragma unroll 1
    for (int p = 0; p < 2; ++p) {
      const auto rv = __builtin_amdgcn_make_buffer_rsrc((void*)(p ? pv1 : pv0), 0, (int)REGION, 0x00020000);
#pragma unroll 1
      for (; it < 8; ++it) {
#pragma unroll
        for (int j = 0; j < 4; ++j) {
          if ((j & 1) == 0) gate_ctl(it < slow_end, j == 0); else gate();
          issue(R[j], rk);
          score(R[(j + 4 - WIN) & 3], (j + 4 - WIN) & 3, j < WIN ? it - 1 : it);
        }
      }
#pragma unroll
      for (int j = 0; j < WIN; ++j) {
        gate();
        issue(R[j], rv);
        score(R[4 - WIN + j], 4 - WIN + j, 7);
      }
      mx = fmaxf(mx, dpp<XOR1>(mx)); mx = fmaxf(mx, dpp<XOR2>(mx)); mx = fmaxf(mx, dpp<XOR7>(mx)); mx = fmaxf(mx, dpp<XOR8>(mx));
      mx = xrow_max(mx);
      const float m2 = mx * C2;
      {
        float l = 0.f;
#pragma unroll
        for (int u = 0; u < 8; ++u) {
          const float e = __builtin_amdgcn_exp2f(fmaf(sc[u * 64], C2, -m2));
          sc[u * 64] = __uint_as_float(pack_bf16(e, e));
          l += e;
        }
        l += dpp<XOR1>(l); l += dpp<XOR2>(l); l += dpp<XOR7>(l); l += dpp<XOR8>(l);
        l = swap16_add(l, l);
        l = swap32_add(l, l);
        if (lane == 0) {
          *(LAS f32x2*)(lds + L_ML + 4 * wave) = f32x2{m2, l};
          wait_lgkm0();
          *(volatile LAS unsigned*)(lds + L_ML + 4 * wave + 2) = p + 1;
        }
      }
      f32x4 acc = {0.f, 0.f, 0.f, 0.f};
      u32x4 a0, a1, a2, a3;
#define PVA(J) do { a0 = diag_of<0 + J>(pr, diag); a1 = diag_of<4 + J>(pr, diag); a2 = diag_of<8 + J>(pr, diag); a3 = diag_of<12 + J>(pr, diag); \
    asm volatile("" : "+v"(a0), "+v"(a1), "+v"(a2), "+v"(a3)); } while (0)
#define PVB(RR) do { acc = rows_mac(acc, a0, RR[0]); acc = rows_mac(acc, a1, RR[1]); acc = rows_mac(acc, a2, RR[2]); acc = rows_mac(acc, a3, RR[3]); \
    asm volatile("" : "+v"(acc)); } while (0)
      unsigned pr = __float_as_uint(sc[0]);
#pragma unroll 1
      for (int u = 0; u < 7; ++u) {
        const unsigned pn = __float_as_uint(sc[(u + 1) * 64]);
        if (u == 6 && wave == 0 && lane == 0) *(volatile LAS unsigned*)(lds + L_CTL + 1) = p + 1;
        PVA(0); gate_ctl(false, true); issue(R[(0 + WIN) & 3], rv); PVB(R[0]);
        PVA(1); gate(); issue(R[(1 + WIN) & 3], rv); PVB(R[1]);
        PVA(2); gate_ctl(false, false); issue(R[(2 + WIN) & 3], rv); PVB(R[2]);
        PVA(3); gate(); issue(R[(3 + WIN) & 3], rv); PVB(R[3]);
        pr = pn;
      }
      rk = __builtin_amdgcn_make_buffer_rsrc((void*)pk1, 0, p ? 0 : (int)REGION, 0x00020000);
      auto gate_end = [&]() {
        PIN();
        wait_vm<4 * (WIN - 1)>();
        if (p == 0) while ((int)((now() << 8) - tq) < 0) __builtin_amdgcn_s_sleep(1);
        PIN();
      };
      PVA(0); gate(); issue(R[(0 + WIN) & 3], rv); PVB(R[0]);
      PVA(1);
      if (WIN == 2) { gate(); issue(R[3], rv); }
      if (p) STAMP(4);
      if (WIN == 3) { gate_end(); issue(R[0], rk); }
      PVB(R[1]);
      PVA(2); gate_end(); issue(R[WIN - 2], rk); PVB(R[2]);
      PVA(3); gate_end(); issue(R[WIN - 1], rk); PVB(R[3]);
#undef PVA
#undef PVB
      if (lane < 32) *(LAS f32x4*)(lds + L_OP + wave * 128 + 8 * c + 4 * g) = acc;
      wait_lgkm0();
      if (p) STAMP(5); else STAMP(3);
      __builtin_amdgcn_s_barrier();
      PIN();
      if (p) break;
      q = *(LAS const u32x4*)(lds + L_Q + 64 + 4 * c);
      mx = -INFINITY;
#pragma unroll
      for (int j = WIN; j < 4; ++j) {
        gate();
        issue(R[j], rk);
        score(R[j - WIN], j - WIN, 0);
      }
      it = 1;
      slow_end = 0;
    }
    STAMP_FLUSH();
    return;
  }

  const int hw = wave - 4;
  STAMP(1);
  __builtin_amdgcn_s_barrier();
  PIN();
  const u32x2 tg2 = *(LAS const u32x2*)(lds + L_TAG);
  const unsigned tlo = __builtin_amdgcn_readfirstlane(tg2[0]), thi = __builtin_amdgcn_readfirstlane(tg2[1]);
  const unsigned yoff = WS_Y + (b * 128 + mem) * 4096 + hw * 1024 + c * 64 + g * 16;
  const unsigned gath = WS_Y + (b * 128 + (lane >> 1)) * 4096 + mem * 32 + (lane & 1) * 16;
  constexpr unsigned GSTR = 32 * 4096;
  auto stats_ready = [&](int p) { return (bool)__all(*(volatile LAS unsigned*)(lds + L_ML + 4 * (lane & 3) + 2) == (unsigned)p + 1u); };
  auto stats = [&](float (&f)[4], float& m, float& l) {
    asm volatile("" ::: "memory");
    float mw[4], lw[4];
#pragma unroll
    for (int w = 0; w < 4; ++w) { const f32x2 ml = *(volatile LAS f32x2*)(lds + L_ML + 4 * w); mw[w] = ml[0]; lw[w] = ml[1]; }
    m = round24(fmaxf(fmaxf(mw[0], mw[1]), fmaxf(mw[2], mw[3])));
    l = 0.f;
#pragma unroll
    for (int w = 0; w < 4; ++w) { f[w] = __builtin_amdgcn_exp2f(mw[w] - m); l = fmaf(f[w], lw[w], l); }
  };
  const unsigned ytag0 = tlo & 0xffffffu, ytag1 = (5u * tlo + ((tlo >> 24) | (thi << 8)) + 1u) & 0xffffffu;
  unsigned tr = ((t0 + AD_T0) >> AD_LGP) << AD_LGP, tc = tr;
  float c_int = 0.f, c_prev = 0.f;
  auto control = [&]() {
    if ((bid & 0xd8) == 0 && (int)(now() - tr) >= 0) {
      float a = (float)*(volatile LAS int*)(lds + L_LATE + (lane & 3) * 64);
      a += dpp<XOR1>(a); a += dpp<XOR2>(a);
      const u32x4 gr = granules(a * (0.25f / 256.f), 0.f, tlo, thi);
      if (lane == 0) __builtin_amdgcn_raw_buffer_store_b64(u32x2{gr[0], gr[1]}, rw, WS_CTL + (((tr >> AD_LGP) & 63u) * 2 + b) * LINE + (bid & 7) * 8, 0, SC1);
      tr = ((now() >> AD_LGP) + 1u) << AD_LGP;
    }
    if ((int)(now() - tc) >= AD_D) {
      const u32x16 e = sload16(pws + WS_CTL + (((tc >> AD_LGP) & 63u) * 2 + b) * LINE);
      float n = 0.f, hi = 0.f, lo = 0.f;
#pragma unroll
      for (int k = 0; k < 8; ++k) {
        const bool v0 = e[2 * k] == tlo && (e[2 * k + 1] & 0xffu) == thi;
        const float l0 = v0 ? val(e[2 * k + 1]) : 0.f;
        n += v0 ? 1.f : 0.f;
        lo = fmaxf(lo, fminf(hi, l0));
        hi = fmaxf(hi, l0);
      }
      const float err = 0.5f * (hi + lo);
      const float pterm = fmaxf(err - AD_CALM, 0.f) * (1.f / AD_TP);
      if (n >= 5.f) {
        if (err < AD_CALM) c_int = fmaxf(c_int - AD_LEAK, 0.f);
        else if ((int)(tc - t0) > AD_TI && err > c_prev + AD_RISE) c_int += AD_STEP;
        c_prev = err;
        if (lane == 0) *(volatile LAS unsigned*)(lds + L_CTL) = (unsigned)((float)PACE_DQ * (1.f + fminf(c_int + pterm, AD_MAX)));
      }
      tc += 1u << AD_LGP;
    }
  };
  unsigned tw = t0 + WO_T0;
  u32x2 ya = {0u, 0u}, yb = {0u, 0u};
#pragma unroll 1
  for (int p = 0; p < 2; ++p) {
    const auto ro = __builtin_amdgcn_make_buffer_rsrc((void*)(p ? pwo1 : pwo0), 0, DM * NH * DH * 2, 0x00020000);
    const unsigned wvo = (unsigned)(h * DH + 2 * g) * (DM * 2) + hw * 256 + c * 16;
    const unsigned gap = p ? WO_GAP1 : WO_GAP0;
    tw += (h * gap) >> 5;
    int ml_state = 0;
    unsigned ml_time = 0;
    auto ml_step = [&](bool last) {
      if (ml_state == 0 && stats_ready(p)) {
        float f[4], m, l;
        stats(f, m, l);
        if (lane == 0) __builtin_amdgcn_raw_buffer_store_b128(granules(m, l, tlo, thi), rw, WS_ML + (p * NWG + bid) * LINE, 0, SC1);
        ml_state = 1;
        ml_time = now();
      } else if (ml_state == 1 && (last || now() - ml_time > 400u)) {
        const u32x4 e = __builtin_amdgcn_raw_buffer_load_b128(rw, WS_ML + (p * NWG + (bid ^ ((lane & 3) << 3))) * LINE, 0, SC1);
        ml_time = now();
        if (__all(valid(e, tlo, thi))) {
          const float m0 = val(e[1]);
          float M = fmaxf(m0, dpp<XOR1>(m0));
          M = fmaxf(M, dpp<XOR2>(M));
          const float e0 = __builtin_amdgcn_exp2f(m0 - M);
          float L = e0 * val(e[3]);
          L += dpp<XOR1>(L);
          L += dpp<XOR2>(L);
          if (lane == 0) {
            *(volatile LAS float*)(lds + L_SCL) = e0 / L;
            wait_lgkm0();
            *(volatile LAS unsigned*)(lds + L_SCL + 1) = p + 1;
          }
          ml_state = 2;
        }
      }
    };
#pragma unroll 1
    for (int t = 0; t < 32; ++t) {
      while ((int)(now() - tw) < 0) {
        if (ADAPT && hw == 1) control();
        __builtin_amdgcn_s_sleep(4);
      }
      tw += gap;
      __builtin_amdgcn_raw_ptr_buffer_load_lds(ro, (LAS void*)(lds + L_WO + (hw * 32 + t) * 256), 16, wvo, ((t >> 1) * 8 + (t & 1)) * (DM * 2), 0, WO_AUX);
      if (hw == 0) {
        ml_step(false);
        if (p == 0 && t == 6) {
          gather_q(1);
          if (sub == 0 && lane == 0 && ok)
            __hip_atomic_store((GAS unsigned long long*)(pws + WS_EH + h * LINE), ((unsigned long long)qhi << 32) | qlo, __ATOMIC_RELAXED, __HIP_MEMORY_SCOPE_AGENT);
        }
      }
      if (hw == 2) {
        if (t == 2) sload2(p ? pv1 : pv0);
        if (t == 16) sload2(p ? pout : pk1);
      }
    }
    if (p) STAMP(2);
    float f[4];
    {
      const unsigned ts = now();
      if (hw == 0) while (ml_state != 2 && now() - ts <= SPIN_LIMIT) { ml_step(true); if (ml_state != 2) __builtin_amdgcn_s_sleep(16); }
      while (*(volatile LAS unsigned*)(lds + L_SCL + 1) != (unsigned)p + 1u && now() - ts <= SPIN_LIMIT) __builtin_amdgcn_s_sleep(4);
      float m, l;
      stats(f, m, l);
      const float scl = (now() - ts <= SPIN_LIMIT && ok) ? *(volatile LAS float*)(lds + L_SCL) : __builtin_nanf("");
#pragma unroll
      for (int w = 0; w < 4; ++w) f[w] *= scl;
    }
    asm volatile("s_waitcnt vmcnt(0)" ::: "memory");
    u32x4 W[32];
#pragma unroll
    for (int u = 0; u < 16; ++u) {
      const u32x4 r0 = *(LAS const u32x4*)(lds + L_WO + (hw * 32 + 2 * u) * 256 + lane * 4), r1 = *(LAS const u32x4*)(lds + L_WO + (hw * 32 + 2 * u + 1) * 256 + lane * 4);
#pragma unroll
      for (int k = 0; k < 2; ++k) {
        W[2 * u][2 * k] = __builtin_amdgcn_perm(r1[k], r0[k], 0x05040100u);
        W[2 * u][2 * k + 1] = __builtin_amdgcn_perm(r1[k], r0[k], 0x07060302u);
        W[2 * u + 1][2 * k] = __builtin_amdgcn_perm(r1[k + 2], r0[k + 2], 0x05040100u);
        W[2 * u + 1][2 * k + 1] = __builtin_amdgcn_perm(r1[k + 2], r0[k + 2], 0x07060302u);
      }
    }
#pragma unroll
    for (int t = 0; t < 32; ++t) asm volatile("" : "+v"(W[t]));
    float y[8];
#pragma unroll
    for (int k = 0; k < 8; ++k) y[k] = 0.f;
    asm volatile("" : "+v"(f[0]), "+v"(f[1]), "+v"(f[2]), "+v"(f[3]), "+v"(y[0]), "+v"(y[1]), "+v"(y[2]), "+v"(y[3]), "+v"(y[4]), "+v"(y[5]), "+v"(y[6]), "+v"(y[7]));
    wait_lgkm0();
    if (ADAPT && hw == 1) {
      const unsigned ts = now();
      while (*(volatile LAS unsigned*)(lds + L_CTL + 1) != (unsigned)p + 1u && now() - ts <= SPIN_LIMIT) { control(); __builtin_amdgcn_s_sleep(4); }
    }
    if (p) { STAMP(3); __builtin_amdgcn_s_setprio(3); }
    __builtin_amdgcn_s_barrier();
    PIN();
    if (p) STAMP(4);
    f32x2 o = f[0] * *(LAS const f32x2*)(lds + L_OP + 8 * c + 2 * g);
#pragma unroll
    for (int w = 1; w < 4; ++w) o += f[w] * *(LAS const f32x2*)(lds + L_OP + w * 128 + 8 * c + 2 * g);
    const unsigned op = pack_bf16(o[0], o[1]);
#define O1(U) { const unsigned ob = dppu<BCAST0 + (U)>(op); _Pragma("unroll") for (int k = 0; k < 4; ++k) { y[k] = dot2(W[2 * (U)][k], ob, y[k]); y[4 + k] = dot2(W[2 * (U) + 1][k], ob, y[4 + k]); } }
    O1(0) O1(1) O1(2) O1(3) O1(4) O1(5) O1(6) O1(7) O1(8) O1(9) O1(10) O1(11) O1(12) O1(13) O1(14) O1(15)
#undef O1
    const f32x2 yy = fold_rows(y);
    ya = yb;
    yb = pair_granule(yy[0], yy[1], p ? ytag1 : ytag0);
    PIN();
    if (p == 0) tw = now();
  }
  __builtin_amdgcn_raw_buffer_store_b128(u32x4{ya[0], ya[1], yb[0], yb[1]}, rw, yoff, 0, SC1);
  STAMP(5);
  if (hw != 0) { STAMP_FLUSH(); return; }
  {
    u32x4 e[6][4];
    float s0 = 0.f, s1 = 0.f;
    auto fetch = [&](u32x4 (&d)[4]) {
#pragma unroll
      for (int k = 0; k < 4; ++k) d[k] = __builtin_amdgcn_raw_buffer_load_b128(rw, gath, k * GSTR, SC1);
      PIN();
    };
    auto there = [&](const u32x4& d) { return (((d[0] ^ ytag0) | (d[2] ^ ytag1)) & 0xffffffu) == 0u; };
    auto take = [&](const u32x4 (&d)[4]) {
      float a0 = 0.f, a1 = 0.f, b0 = 0.f, b1 = 0.f;
#pragma unroll
      for (int k = 0; k < 4; ++k) { a0 += pair_first(d[k][1]); a1 += pair_second(d[k][0], d[k][1]); b0 += pair_first(d[k][3]); b1 += pair_second(d[k][2], d[k][3]); }
      a0 += dpp<XOR2>(a0); a1 += dpp<XOR2>(a1); b0 += dpp<XOR2>(b0); b1 += dpp<XOR2>(b1);
      s0 = (lane & 2) ? b0 : a0;
      s1 = (lane & 2) ? b1 : a1;
      return (bool)__all((int)there(d[0]) & (int)there(d[1]) & (int)there(d[2]) & (int)there(d[3]));
    };
    __builtin_amdgcn_s_sleep(POLL_T0);
    fetch(e[0]); __builtin_amdgcn_s_sleep(POLL_GAP);
    fetch(e[1]); __builtin_amdgcn_s_sleep(POLL_GAP);
    fetch(e[2]);
    bool all = take(e[0]);
    if (!all) { fetch(e[3]); all = take(e[1]);
      if (!all) { fetch(e[4]); all = take(e[2]);
        if (!all) { fetch(e[5]); all = take(e[3]);
          if (!all) { all = take(e[4]);
            if (!all) {
              const unsigned ts = now();
              while (!(all = take(e[5])) && now() - ts <= SPIN_LIMIT) {
                __builtin_amdgcn_s_sleep(4);
#pragma unroll
                for (int k = 0; k < 4; ++k) e[4][k] = __builtin_amdgcn_raw_buffer_load_b128(rw, there(e[5][k]) ? WS_END : gath + k * GSTR, 0, SC1);
#pragma unroll
                for (int k = 0; k < 4; ++k) if (!there(e[5][k])) e[5][k] = e[4][k];
              }
            } } } } }
    s0 += dpp<XOR8>(s0); s1 += dpp<XOR8>(s1);
    s0 += dpp<ROR4>(s0); s1 += dpp<ROR4>(s1);
    s0 = swap16_add(s0, s0); s1 = swap16_add(s1, s1);
    s0 = swap32_add(s0, s0); s1 = swap32_add(s1, s1);
    if (!(ok && all)) { s0 = __builtin_nanf(""); s1 = s0; }
    if (lane < 4) ((GAS unsigned*)pout)[(b * 2 * DM + (lane >> 1) * DM + mem * 4) / 2 + (lane & 1)] = pack_bf16(s0, s1);
    STAMP(6);
    if (mem == 0 && lane == 0 && all)
      __hip_atomic_store((GAS unsigned long long*)(pws + WS_EG + b * LINE), ((unsigned long long)thi << 32) | tlo, __ATOMIC_RELAXED, __HIP_MEMORY_SCOPE_AGENT);
    PIN();
#pragma unroll
    for (int i = 0; i < 6; ++i) asm volatile("" :: "v"(e[i][0]), "v"(e[i][1]), "v"(e[i][2]), "v"(e[i][3]));
  }
  STAMP(11);
  STAMP_FLUSH();
}

extern "C" void kernel_launch(void* const* d_in, const int* in_sizes, int n_in, void* d_out, int out_size, void* d_ws,
                              size_t ws_size, hipStream_t stream) {
  Params P;
  for (int p = 0; p < 2; ++p) {
    P.x[p] = (const unsigned char*)d_in[5 * p + 0];
    P.k[p] = (const unsigned char*)d_in[5 * p + 1];
    P.v[p] = (const unsigned char*)d_in[5 * p + 2];
    P.wq[p] = (const unsigned char*)d_in[5 * p + 3];
    P.wo[p] = (const unsigned char*)d_in[5 * p + 4];
  }
  P.out = (unsigned char*)d_out;
  P.ws = (unsigned char*)d_ws + ((ws_size - WS_END - (size_t)WS_AT_END * 1048576) & ~(size_t)0x1fffff);
  mha_decode_kernel<<<NWG, NTHR, 0, stream>>>(P);
}
```

```cpp
#include <hip/hip_runtime.h>
#include <hip/hip_bf16.h>
#include <stdint.h>

typedef unsigned u32x2 __attribute__((ext_vector_type(2)));
typedef unsigned u32x4 __attribute__((ext_vector_type(4)));
typedef unsigned u32x16 __attribute__((ext_vector_type(16)));
typedef float f32x2 __attribute__((ext_vector_type(2)));
typedef float f32x4 __attribute__((ext_vector_type(4)));
typedef __bf16 bf16x2 __attribute__((ext_vector_type(2)));
typedef __bf16 bf16x8 __attribute__((ext_vector_type(8)));
#define LAS __attribute__((address_space(3)))
#define GAS __attribute__((address_space(1)))

constexpr unsigned PACE_DQ = 13700;
constexpr unsigned PACE_DQ1 = 14500;
constexpr unsigned PACE_T0 = 240;
constexpr unsigned Q_DELAY = 200;
constexpr unsigned WO_PARK = 180;
constexpr unsigned WO_T0 = 1700, WO_GAP0 = 52, WO_GAP1 = 80;
constexpr int POLL_T0 = 18, POLL_GAP = 6;
constexpr unsigned AD_LGP = 8;
constexpr int AD_D = 180;
constexpr unsigned AD_T0 = 1100;
constexpr int AD_TI = 1500;
constexpr float AD_CALM = 135.f;
constexpr float AD_TP = 1200.f;
constexpr float AD_RISE = 10.f, AD_STEP = 0.006f, AD_LEAK = 0.0015f, AD_MAX = 0.2f;

constexpr int NH = 32, DH = 128, DM = 512;
constexpr int NWG = 256, NTHR = 512;
constexpr unsigned REGION = 1u << 19;
constexpr int NT = 2, SC1 = 16;
constexpr float C2 = 0.08838834764831845f * 1.4426950408889634f;

constexpr unsigned LINE = 256;
constexpr unsigned WS_EG = 0, WS_EH = WS_EG + 2 * LINE, WS_ML = WS_EH + 32 * LINE, WS_CTL = WS_ML + 2 * NWG * LINE, WS_QX = WS_CTL + 128 * LINE,
                   WS_Y = WS_QX + 32 * 8192, WS_END = WS_Y + NWG * 4096;
constexpr int L_SC = 0;
constexpr int L_OP = L_SC + 2048;
constexpr int L_Q = L_OP + 1024;
constexpr int L_ML = L_Q + 128;
constexpr int L_TAG = L_ML + 16;
constexpr int L_CTL = L_TAG + 2;
constexpr int L_SCL = L_TAG + 4;
constexpr int L_LATE = L_TAG + 8;
constexpr int L_WO = L_LATE + 256;
constexpr int L_END = L_WO + 32768;

struct Params {
  const unsigned char* x[2];
  const unsigned char* k[2];
  const unsigned char* v[2];
  const unsigned char* wq[2];
  const unsigned char* wo[2];
  unsigned char* out;
  unsigned char* ws;
};

__device__ __forceinline__ float dot2(unsigned a, unsigned b, float acc) {
  return __builtin_amdgcn_fdot2_f32_bf16(__builtin_bit_cast(bf16x2, a), __builtin_bit_cast(bf16x2, b), acc, false);
}
template <int CTRL> __device__ __forceinline__ unsigned dppu(unsigned x) { return (unsigned)__builtin_amdgcn_mov_dpp((int)x, CTRL, 0xf, 0xf, true); }
template <int CTRL> __device__ __forceinline__ float dpp(float x) { return __builtin_bit_cast(float, dppu<CTRL>(__builtin_bit_cast(unsigned, x))); }
constexpr int XOR1 = 0xB1, XOR2 = 0x4E, XOR7 = 0x141, XOR8 = 0x128, ROR4 = 0x124, BCAST0 = 0x150;
__device__ __forceinline__ float swap16_add(float a, float b) {
  auto s = __builtin_amdgcn_permlane16_swap(__float_as_uint(a), __float_as_uint(b), false, false);
  return __uint_as_float(s[0]) + __uint_as_float(s[1]);
}
__device__ __forceinline__ float swap32_add(float a, float b) {
  auto s = __builtin_amdgcn_permlane32_swap(__float_as_uint(a), __float_as_uint(b), false, false);
  return __uint_as_float(s[0]) + __uint_as_float(s[1]);
}
__device__ __forceinline__ float xrow_max(float x) {
  auto s = __builtin_amdgcn_permlane16_swap(__float_as_uint(x), __float_as_uint(x), false, false);
  x = fmaxf(__uint_as_float(s[0]), __uint_as_float(s[1]));
  auto t = __builtin_amdgcn_permlane32_swap(__float_as_uint(x), __float_as_uint(x), false, false);
  return fmaxf(__uint_as_float(t[0]), __uint_as_float(t[1]));
}
__device__ __forceinline__ f32x2 fold_rows(const float (&a)[8]) {
  const float z0 = swap32_add(a[0], a[4]), z1 = swap32_add(a[1], a[5]), z2 = swap32_add(a[2], a[6]), z3 = swap32_add(a[3], a[7]);
  return f32x2{swap16_add(z0, z2), swap16_add(z1, z3)};
}
__device__ __forceinline__ unsigned pack_bf16(float lo, float hi) {
  bf16x2 r;
  r[0] = (__bf16)lo;
  r[1] = (__bf16)hi;
  return __builtin_bit_cast(unsigned, r);
}
template <int T> __device__ __forceinline__ u32x4 diag_of(unsigned w, const u32x4& diag) {
  return u32x4{dppu<BCAST0 + T>(w) & diag[0], dppu<BCAST0 + T>(w) & diag[1], dppu<BCAST0 + T>(w) & diag[2], dppu<BCAST0 + T>(w) & diag[3]};
}
__device__ __forceinline__ f32x4 rows_mac(f32x4 acc, const u32x4& a, const u32x4& v) {
  return __builtin_amdgcn_mfma_f32_16x16x32_bf16(__builtin_bit_cast(bf16x8, a), __builtin_bit_cast(bf16x8, v), acc, 0, 0, 0);
}
__device__ __forceinline__ u32x4 diag_mask(int c) {
  const unsigned half = (c & 1) ? 0xffff0000u : 0x0000ffffu;
  return u32x4{(c >> 1) == 0 ? half : 0u, (c >> 1) == 1 ? half : 0u, (c >> 1) == 2 ? half : 0u, (c >> 1) == 3 ? half : 0u};
}
__device__ __forceinline__ float round24(float v) { return __uint_as_float((__float_as_uint(v) + 0x80u) & 0xffffff00u); }
__device__ __forceinline__ u32x4 granules(float v0, float v1, unsigned tlo, unsigned thi) {
  return u32x4{tlo, __float_as_uint(round24(v0)) | thi, tlo, __float_as_uint(round24(v1)) | thi};
}
__device__ __forceinline__ bool valid(const u32x4& e, unsigned tlo, unsigned thi) {
  return (((e[0] ^ tlo) | (e[2] ^ tlo)) | (((e[1] ^ thi) | (e[3] ^ thi)) & 0xffu)) == 0u;
}
__device__ __forceinline__ float val(unsigned u) { return __uint_as_float(u & 0xffffff00u); }
__device__ __forceinline__ u32x2 pair_granule(float v0, float v1, unsigned tag24) {
  const unsigned r0 = (__float_as_uint(v0) + 0x800u) & 0xfffff000u, r1 = __float_as_uint(v1) + 0x800u;
  return u32x2{((r1 << 12) & 0xff000000u) | tag24, r0 | (r1 >> 20)};
}
__device__ __forceinline__ float pair_first(unsigned hi) { return __uint_as_float(hi & 0xfffff000u); }
__device__ __forceinline__ float pair_second(unsigned lo, unsigned hi) { return __uint_as_float((hi << 20) | ((lo >> 12) & 0xff000u)); }
__device__ __forceinline__ unsigned pair_tag0(unsigned tlo) { return tlo & 0xffffffu; }
__device__ __forceinline__ unsigned pair_tag1(unsigned tlo, unsigned thi) { return (5u * tlo + ((tlo >> 24) | (thi << 8)) + 1u) & 0xffffffu; }
__device__ __forceinline__ bool pair_valid(const u32x4& d, unsigned tag0, unsigned tag1) { return (((d[0] ^ tag0) | (d[2] ^ tag1)) & 0xffffffu) == 0u; }
template <int N> __device__ __forceinline__ void wait_vm() { __builtin_amdgcn_s_waitcnt((N & 15) | ((N >> 4) << 14) | (7 << 4) | (15 << 8)); }
__device__ __forceinline__ void wait_lgkm0() { __builtin_amdgcn_s_waitcnt(0xC07F); }
__device__ __forceinline__ unsigned now() { return (unsigned)__builtin_amdgcn_s_memrealtime(); }
__device__ __forceinline__ u32x2 sload2(const unsigned char* p) {
  u32x2 e;
  asm volatile("s_load_dwordx2 %0, %1, 0x0\n\ts_waitcnt lgkmcnt(0)" : "=&s"(e) : "s"(p) : "memory");
  return e;
}
__device__ __forceinline__ u32x16 sload16(const unsigned char* p) {
  u32x16 e;
  asm volatile("s_load_dwordx16 %0, %1, 0x0\n\ts_waitcnt lgkmcnt(0)" : "=&s"(e) : "s"(p) : "memory");
  return e;
}
#define PIN() __builtin_amdgcn_sched_barrier(0)
__device__ __forceinline__ void lds_barrier() {
  asm volatile("" ::: "memory");
  wait_lgkm0();
  __builtin_amdgcn_s_barrier();
  asm volatile("" ::: "memory");
  PIN();
}
constexpr unsigned SPIN_LIMIT = 5000000u;

__global__ void __launch_bounds__(NTHR, 2) mha_decode_kernel(Params P) {
  __shared__ __attribute__((aligned(16))) float lds_raw[L_END];
  LAS float* lds = (LAS float*)lds_raw;
  const unsigned t0 = now();
  PIN();
  const int bid = blockIdx.x;
  const int h = (bid & 7) * 4 + (bid >> 6), sub = (bid >> 3) & 7;
  const int b = sub >> 2, qd = sub & 3;
  const int lane = threadIdx.x & 63;
  const int wave = __builtin_amdgcn_readfirstlane(threadIdx.x >> 6);
  const int g = lane >> 4, c = lane & 15;
  const unsigned mem = 4 * h + qd;

  const size_t rofs = (size_t)(b * 128 + mem) * REGION;
  const unsigned char *px = b ? P.x[1] : P.x[0], *pwq = b ? P.wq[1] : P.wq[0];
  const unsigned char *pk0 = P.k[0] + rofs, *pk1 = P.k[1] + rofs, *pv0 = P.v[0] + rofs, *pv1 = P.v[1] + rofs, *pwo0 = P.wo[0], *pwo1 = P.wo[1];
  unsigned char *pout = P.out, *pws = P.ws;
  asm volatile("" : "+s"(px), "+s"(pwq), "+s"(pk0), "+s"(pk1), "+s"(pv0), "+s"(pv1), "+s"(pwo0), "+s"(pwo1), "+s"(pout), "+s"(pws));

  const unsigned voff = lane * 16 + ((wave & 1) << 13) + ((wave & 2) << 14);
  unsigned pos = 2 * h + (qd >> 1) + 4 * (qd & 1);
  unsigned tq = (t0 + PACE_T0) << 8;
  unsigned dq = PACE_DQ1;
  u32x4 R[12][4];
  auto issue = [&](u32x4 (&dst)[4], const __amdgpu_buffer_rsrc_t& r) {
    const unsigned s = ((pos & 4u) << 10) | ((pos & 1u) << 14) | ((pos & 2u) << 15) | ((pos & 24u) << 14);
#pragma unroll
    for (int t = 0; t < 4; ++t) dst[t] = __builtin_amdgcn_raw_buffer_load_b128(r, voff + t * 1024, s, NT);
    pos += 1;
    tq += dq;
    PIN();
  };
  auto rk = __builtin_amdgcn_make_buffer_rsrc((void*)pk0, 0, (int)REGION, 0x00020000);
  if (wave < 4) issue(R[0], rk);
  int role = wave;
  asm volatile("" : "+s"(role));

  const auto rw = __builtin_amdgcn_make_buffer_rsrc((void*)pws, 0, (int)WS_END, 0x00020000);
  bool ok = true;
  unsigned qlo = 0, qhi = 0;
  auto gather_q = [&](int p) {
    const unsigned ts = now(), tag0 = pair_tag0(qlo), tag1 = pair_tag1(qlo, qhi);
    const unsigned src = WS_QX + h * 8192 + (4 * p + (lane >> 5)) * 1024 + b * 512 + (lane & 31) * 16;
    u32x4 e0, e1;
    bool all;
    do {
      e0 = __builtin_amdgcn_raw_buffer_load_b128(rw, src, 0, SC1);
      e1 = __builtin_amdgcn_raw_buffer_load_b128(rw, src, 2048, SC1);
      all = __all((int)pair_valid(e0, tag0, tag1) & (int)pair_valid(e1, tag0, tag1));
      if (!all) __builtin_amdgcn_s_sleep(16);
    } while (!all && now() - ts <= SPIN_LIMIT);
    ok = ok && all;
    float v0 = pair_first(e0[1]) + pair_first(e1[1]), v1 = pair_second(e0[0], e0[1]) + pair_second(e1[0], e1[1]);
    float v2 = pair_first(e0[3]) + pair_first(e1[3]), v3 = pair_second(e0[2], e0[3]) + pair_second(e1[2], e1[3]);
    v0 = swap32_add(v0, v0); v1 = swap32_add(v1, v1); v2 = swap32_add(v2, v2); v3 = swap32_add(v3, v3);
    if (lane < 32) *(LAS u32x2*)(lds + L_Q + p * 64 + 4 * c + 2 * g) = u32x2{pack_bf16(v0, v1), pack_bf16(v2, v3)};
  };
  auto park_wo = [&](const unsigned char* pw) {
    const auto r = __builtin_amdgcn_make_buffer_rsrc((void*)(pw + h * 131072 + sub * 16384), 0, 16384, 0x00020000);
    const unsigned d0 = __builtin_amdgcn_raw_buffer_load_b32(r, lane * 128, 0, NT);
    PIN();
    const unsigned e0 = __builtin_amdgcn_raw_buffer_load_b32(r, lane * 128 + 64, 0, SC1);
    PIN();
    const unsigned d1 = __builtin_amdgcn_raw_buffer_load_b32(r, lane * 128, 8192, NT);
    PIN();
    const unsigned e1 = __builtin_amdgcn_raw_buffer_load_b32(r, lane * 128 + 64, 8192, SC1);
    PIN();
    asm volatile("" :: "v"(d0), "v"(e0), "v"(d1), "v"(e1));
  };
  if (role == 4) {
    const auto rq = __builtin_amdgcn_make_buffer_rsrc((void*)(pwq + (size_t)qd * 128 * (NH * DH * 2) + h * (DH * 2)), 0, 128 * NH * DH * 2 - h * (DH * 2), 0x00020000);
    const auto rx = __builtin_amdgcn_make_buffer_rsrc((void*)px, 0, 2 * DM * 2, 0x00020000);
    const unsigned xo = (128 * qd + 4 * c + g) * 2;
    const unsigned x00 = __builtin_amdgcn_raw_buffer_load_b16(rx, xo, 0, NT), x01 = __builtin_amdgcn_raw_buffer_load_b16(rx, xo + DM * 2, 0, NT);
    const unsigned x10 = __builtin_amdgcn_raw_buffer_load_b16(rx, xo + 128, 0, NT), x11 = __builtin_amdgcn_raw_buffer_load_b16(rx, xo + 128 + DM * 2, 0, NT);
    u32x4 W[32];
    PIN();
    const unsigned vo = g * (NH * DH * 2) + c * 16;
#pragma unroll
    for (int t = 0; t < 32; ++t) W[t] = __builtin_amdgcn_raw_buffer_load_b128(rq, vo, t * 4 * (NH * DH * 2), NT);
    PIN();
    const u32x2 prev = sload2(pws + WS_EH + h * LINE);
    qlo = prev[0] + 1u;
    qhi = (prev[1] + (qlo == 0u ? 1u : 0u)) & 0xffu;
    if (lane < 4) *(volatile LAS unsigned*)(lds + L_ML + 4 * lane + 2) = 0u;
    const unsigned xa = x00 | (x01 << 16), xb = x10 | (x11 << 16);
    unsigned sel[4];
#pragma unroll
    for (int k = 0; k < 4; ++k) {
      const unsigned pick = (c & 8) ? 0x0302u : 0x0100u;
      sel[k] = ((c & 7) == 2 * k ? pick : 0x0c0cu) | (((c & 7) == 2 * k + 1 ? pick : 0x0c0cu) << 16);
    }
    f32x4 d0 = {0.f, 0.f, 0.f, 0.f}, d1 = {0.f, 0.f, 0.f, 0.f};
#define QM(T, X, D) { const unsigned xt = dppu<BCAST0 + ((T) & 15)>(X); \
      const u32x4 a = {__builtin_amdgcn_perm(xt, xt, sel[0]), __builtin_amdgcn_perm(xt, xt, sel[1]), __builtin_amdgcn_perm(xt, xt, sel[2]), __builtin_amdgcn_perm(xt, xt, sel[3])}; \
      D = rows_mac(D, a, W[T]); }
#define QM4(T, X) QM(T, X, d0) QM(T + 1, X, d1) QM(T + 2, X, d0) QM(T + 3, X, d1) PIN();
    QM4(0, xa) QM4(4, xa) QM4(8, xa) QM4(12, xa) QM4(16, xb) QM4(20, xb) QM4(24, xb) QM4(28, xb)
#undef QM4
#undef QM
    d0 += d1;
    const u32x2 g0 = pair_granule(d0[0], d0[1], pair_tag0(qlo)), g1 = pair_granule(d0[2], d0[3], pair_tag1(qlo, qhi));
    __builtin_amdgcn_raw_buffer_store_b128(u32x4{g0[0], g0[1], g1[0], g1[1]}, rw, WS_QX + h * 8192 + sub * 1024 + lane * 16, 0, SC1);
    const unsigned ts = now();
    while (now() - ts < Q_DELAY) __builtin_amdgcn_s_sleep(1);
    gather_q(0);
  } else if (role == 5) {
    const u32x2 prev = sload2(pws + WS_EG + b * LINE);
    const unsigned tlo = prev[0] + 1u, thi = (prev[1] + (tlo == 0u ? 1u : 0u)) & 0xffu;
    if (lane == 0) { *(LAS u32x4*)(lds + L_TAG) = u32x4{tlo, thi, PACE_DQ, 0u}; *(LAS u32x4*)(lds + L_SCL) = u32x4{0u, 0u, 0u, 0u}; }
    *(LAS u32x4*)(lds + L_LATE + 4 * lane) = u32x4{0u, 0u, 0u, 0u};
  } else if (role == 7) {
    while (now() - t0 < WO_PARK) __builtin_amdgcn_s_sleep(1);
    park_wo(pwo0);
    park_wo(pwo1);
  }
  asm volatile("" : "+s"(role));

  if (role < 4) {
    __builtin_amdgcn_s_setprio(3);
    auto gate = [&]() {
      PIN();
      wait_vm<4>();
      while ((int)((now() << 8) - tq) < 0) __builtin_amdgcn_s_sleep(1);
      PIN();
    };
    auto gate_ctl = [&](bool slow, bool report) {
      PIN();
      wait_vm<4>();
      const unsigned ctl = __builtin_amdgcn_readfirstlane(*(volatile LAS unsigned*)(lds + L_CTL));
      if (report) *(volatile LAS int*)(lds + L_LATE + wave * 64 + lane) = (int)((now() << 8) - tq + 2 * dq);
      dq = (slow && ctl < PACE_DQ1) ? PACE_DQ1 : ctl;
      while ((int)((now() << 8) - tq) < 0) __builtin_amdgcn_s_sleep(1);
      PIN();
    };
#pragma unroll
    for (int j = 1; j < 12; ++j) { gate(); issue(R[j], rk); }
    lds_barrier();
    u32x4 q = *(LAS const u32x4*)(lds + L_Q + 4 * c);
    LAS float* sc = lds + L_SC + wave * 512 + lane;
    float keep = 0.f, mx = -INFINITY;
    auto score = [&](const u32x4 (&K)[4], int J, int row) {
      float x[4];
#pragma unroll
      for (int t = 0; t < 4; ++t) {
        float d = dot2(K[t][0], q[0], 0.f);
        d = dot2(K[t][1], q[1], d);
        d = dot2(K[t][2], q[2], d);
        x[t] = dot2(K[t][3], q[3], d);
      }
      const float a0 = x[0] + dpp<XOR8>(x[0]), a1 = x[1] + dpp<XOR8>(x[1]), a2 = x[2] + dpp<XOR8>(x[2]), a3 = x[3] + dpp<XOR8>(x[3]);
      const float y0 = (c & 8) ? a2 : a0, y1 = (c & 8) ? a3 : a1;
      const float b0 = y0 + dpp<XOR7>(y0), b1 = y1 + dpp<XOR7>(y1);
      float s = (c & 4) ? b1 : b0;
      s += dpp<XOR2>(s);
      s += dpp<XOR1>(s);
      keep = ((c & 3) == J) ? s : keep;
      if (J == 3) { sc[row * 64] = keep; mx = fmaxf(mx, keep); }
      asm volatile("" : "+v"(keep), "+v"(mx));
    };
    score(R[0], 0, 0); score(R[1], 1, 0); score(R[2], 2, 0); score(R[3], 3, 0);
    gate_ctl(true, true); issue(R[0], rk);
    score(R[4], 0, 1); score(R[5], 1, 1); score(R[6], 2, 1); score(R[7], 3, 1);
    gate(); issue(R[1], rk);
    score(R[8], 0, 2); score(R[9], 1, 2); score(R[10], 2, 2);
    gate(); issue(R[2], rk);
    score(R[11], 3, 2); score(R[0], 0, 3);
    gate(); issue(R[3], rk);
    score(R[1], 1, 3);
    const u32x4 diag = diag_mask(c);
    int it = 4, slow_end = 5;
#pragma unroll 1
    for (int p = 0; p < 2; ++p) {
      const auto rv = __builtin_amdgcn_make_buffer_rsrc((void*)(p ? pv1 : pv0), 0, (int)REGION, 0x00020000);
#pragma unroll 1
      for (; it < 8; ++it) {
#pragma unroll
        for (int j = 0; j < 4; ++j) {
          if ((j & 1) == 0) gate_ctl(it < slow_end, j == 0); else gate();
          issue(R[j], rk);
          score(R[(j + 2) & 3], (j + 2) & 3, j < 2 ? it - 1 : it);
        }
      }
      gate(); issue(R[0], rv); score(R[2], 2, 7);
      gate(); issue(R[1], rv); score(R[3], 3, 7);
      mx = fmaxf(mx, dpp<XOR1>(mx)); mx = fmaxf(mx, dpp<XOR2>(mx)); mx = fmaxf(mx, dpp<XOR7>(mx)); mx = fmaxf(mx, dpp<XOR8>(mx));
      mx = xrow_max(mx);
      const float m2 = mx * C2;
      {
        float l = 0.f;
#pragma unroll
        for (int u = 0; u < 8; ++u) {
          const float e = __builtin_amdgcn_exp2f(fmaf(sc[u * 64], C2, -m2));
          sc[u * 64] = __uint_as_float(pack_bf16(e, e));
          l += e;
        }
        l += dpp<XOR1>(l); l += dpp<XOR2>(l); l += dpp<XOR7>(l); l += dpp<XOR8>(l);
        l = swap16_add(l, l);
        l = swap32_add(l, l);
        if (lane == 0) {
          *(LAS f32x2*)(lds + L_ML + 4 * wave) = f32x2{m2, l};
          wait_lgkm0();
          *(volatile LAS unsigned*)(lds + L_ML + 4 * wave + 2) = p + 1;
        }
      }
      f32x4 acc = {0.f, 0.f, 0.f, 0.f};
      u32x4 a0, a1, a2, a3;
#define PVA(J) do { a0 = diag_of<0 + J>(pr, diag); a1 = diag_of<4 + J>(pr, diag); a2 = diag_of<8 + J>(pr, diag); a3 = diag_of<12 + J>(pr, diag); \
    asm volatile("" : "+v"(a0), "+v"(a1), "+v"(a2), "+v"(a3)); } while (0)
#define PVB(RR) do { acc = rows_mac(acc, a0, RR[0]); acc = rows_mac(acc, a1, RR[1]); acc = rows_mac(acc, a2, RR[2]); acc = rows_mac(acc, a3, RR[3]); \
    asm volatile("" : "+v"(acc)); } while (0)
      unsigned pr = __float_as_uint(sc[0]);
#pragma unroll 1
      for (int u = 0; u < 7; ++u) {
        const unsigned pn = __float_as_uint(sc[(u + 1) * 64]);
        if (u == 6 && p && wave == 0 && lane == 0) *(volatile LAS unsigned*)(lds + L_CTL + 1) = 2u;
        PVA(0); gate_ctl(false, true); issue(R[2], rv); PVB(R[0]);
        PVA(1); gate(); issue(R[3], rv); PVB(R[1]);
        PVA(2); gate_ctl(false, false); issue(R[0], rv); PVB(R[2]);
        PVA(3); gate(); issue(R[1], rv); PVB(R[3]);
        pr = pn;
      }
      rk = __builtin_amdgcn_make_buffer_rsrc((void*)pk1, 0, p ? 0 : (int)REGION, 0x00020000);
      auto gate_end = [&]() {
        PIN();
        wait_vm<4>();
        if (p == 0) while ((int)((now() << 8) - tq) < 0) __builtin_amdgcn_s_sleep(1);
        PIN();
      };
      PVA(0); gate(); issue(R[2], rv); PVB(R[0]);
      PVA(1); gate(); issue(R[3], rv); PVB(R[1]);
      if (wave == 0 && lane == 0) *(volatile LAS unsigned*)(lds + L_CTL + 1) = p + 1;
      PVA(2); gate_end(); issue(R[0], rk); PVB(R[2]);
      PVA(3); gate_end(); issue(R[1], rk); PVB(R[3]);
#undef PVA
#undef PVB
      if (lane < 32) *(LAS f32x4*)(lds + L_OP + p * 512 + wave * 128 + 8 * c + 4 * g) = acc;
      lds_barrier();
      if (p) break;
      q = *(LAS const u32x4*)(lds + L_Q + 64 + 4 * c);
      mx = -INFINITY;
      gate(); issue(R[2], rk); score(R[0], 0, 0);
      gate(); issue(R[3], rk); score(R[1], 1, 0);
      it = 1;
      slow_end = 0;
    }
    return;
  }

  const int hw = wave - 4;
  lds_barrier();
  const u32x2 tg2 = *(LAS const u32x2*)(lds + L_TAG);
  const unsigned tlo = __builtin_amdgcn_readfirstlane(tg2[0]), thi = __builtin_amdgcn_readfirstlane(tg2[1]);
  unsigned yoff = WS_Y + (b * 128 + mem) * 4096 + hw * 1024 + c * 64 + g * 16;
  unsigned gath = WS_Y + (b * 128 + (lane >> 1)) * 4096 + mem * 32 + (lane & 1) * 16;
  asm volatile("" : "+v"(yoff), "+v"(gath));
  constexpr unsigned GSTR = 32 * 4096;
  auto stats_ready = [&](int p) { return (bool)__all(*(volatile LAS unsigned*)(lds + L_ML + 4 * (lane & 3) + 2) == (unsigned)p + 1u); };
  auto stats = [&](float (&f)[4], float& m, float& l) {
    asm volatile("" ::: "memory");
    float mw[4], lw[4];
#pragma unroll
    for (int w = 0; w < 4; ++w) { const f32x2 ml = *(volatile LAS f32x2*)(lds + L_ML + 4 * w); mw[w] = ml[0]; lw[w] = ml[1]; }
    m = round24(fmaxf(fmaxf(mw[0], mw[1]), fmaxf(mw[2], mw[3])));
    l = 0.f;
#pragma unroll
    for (int w = 0; w < 4; ++w) { f[w] = __builtin_amdgcn_exp2f(mw[w] - m); l = fmaf(f[w], lw[w], l); }
  };
  const unsigned ytag0 = pair_tag0(tlo), ytag1 = pair_tag1(tlo, thi);
  unsigned tr = ((t0 + AD_T0) >> AD_LGP) << AD_LGP, tc = tr;
  float c_int = 0.f, c_prev = 0.f;
  auto control = [&]() {
    if ((bid & 0xd8) == 0 && (int)(now() - tr) >= 0) {
      float a = (float)*(volatile LAS int*)(lds + L_LATE + (lane & 3) * 64);
      a += dpp<XOR1>(a); a += dpp<XOR2>(a);
      const u32x4 gr = granules(a * (0.25f / 256.f), 0.f, tlo, thi);
      if (lane == 0) __builtin_amdgcn_raw_buffer_store_b64(u32x2{gr[0], gr[1]}, rw, WS_CTL + (((tr >> AD_LGP) & 63u) * 2 + b) * LINE + (bid & 7) * 8, 0, SC1);
      tr = ((now() >> AD_LGP) + 1u) << AD_LGP;
    }
    if ((int)(now() - tc) >= AD_D) {
      const u32x16 e = sload16(pws + WS_CTL + (((tc >> AD_LGP) & 63u) * 2 + b) * LINE);
      float n = 0.f, hi = 0.f, lo = 0.f;
#pragma unroll
      for (int k = 0; k < 8; ++k) {
        const bool v0 = e[2 * k] == tlo && (e[2 * k + 1] & 0xffu) == thi;
        const float l0 = v0 ? val(e[2 * k + 1]) : 0.f;
        n += v0 ? 1.f : 0.f;
        lo = fmaxf(lo, fminf(hi, l0));
        hi = fmaxf(hi, l0);
      }
      const float err = 0.5f * (hi + lo);
      const float pterm = fmaxf(err - AD_CALM, 0.f) * (1.f / AD_TP);
      if (n >= 5.f) {
        if (err < AD_CALM) c_int = fmaxf(c_int - AD_LEAK, 0.f);
        else if ((int)(tc - t0) > AD_TI && err > c_prev + AD_RISE) c_int += AD_STEP;
        c_prev = err;
        if (lane == 0) *(volatile LAS unsigned*)(lds + L_CTL) = (unsigned)((float)PACE_DQ * (1.f + fminf(c_int + pterm, AD_MAX)));
      }
      tc += 1u << AD_LGP;
    }
  };
  unsigned tw = t0 + WO_T0;
  u32x2 ya = {0u, 0u}, yb = {0u, 0u};
#pragma unroll 1
  for (int p = 0; p < 2; ++p) {
    const auto ro = __builtin_amdgcn_make_buffer_rsrc((void*)(p ? pwo1 : pwo0), 0, DM * NH * DH * 2, 0x00020000);
    const unsigned wvo = (unsigned)(h * DH + 2 * g) * (DM * 2) + hw * 256 + c * 16;
    const unsigned gap = p ? WO_GAP1 : WO_GAP0;
    tw += (h * gap) >> 5;
    int ml_state = 0;
    unsigned ml_time = 0;
    auto ml_step = [&](bool last) {
      if (ml_state == 0 && stats_ready(p)) {
        float f[4], m, l;
        stats(f, m, l);
        if (lane == 0) __builtin_amdgcn_raw_buffer_store_b128(granules(m, l, tlo, thi), rw, WS_ML + (p * NWG + bid) * LINE, 0, SC1);
        ml_state = 1;
        ml_time = now();
      } else if (ml_state == 1 && (last || now() - ml_time > 400u)) {
        const u32x4 e = __builtin_amdgcn_raw_buffer_load_b128(rw, WS_ML + (p * NWG + (bid ^ ((lane & 3) << 3))) * LINE, 0, SC1);
        ml_time = now();
        if (__all(valid(e, tlo, thi))) {
          const float m0 = val(e[1]);
          float M = fmaxf(m0, dpp<XOR1>(m0));
          M = fmaxf(M, dpp<XOR2>(M));
          const float e0 = __builtin_amdgcn_exp2f(m0 - M);
          float L = e0 * val(e[3]);
          L += dpp<XOR1>(L);
          L += dpp<XOR2>(L);
          if (lane == 0) {
            *(volatile LAS float*)(lds + L_SCL) = e0 / L;
            wait_lgkm0();
            *(volatile LAS unsigned*)(lds + L_SCL + 1) = p + 1;
          }
          ml_state = 2;
        }
      }
    };
#pragma unroll 1
    for (int t = 0; t < 32; ++t) {
      while ((int)(now() - tw) < 0) {
        if (hw == 1) control();
        __builtin_amdgcn_s_sleep(4);
      }
      tw += gap;
      __builtin_amdgcn_raw_ptr_buffer_load_lds(ro, (LAS void*)(lds + L_WO + (hw * 32 + t) * 256), 16, wvo, ((t >> 1) * 8 + (t & 1)) * (DM * 2), 0, NT);
      if (hw == 0) {
        ml_step(false);
        if (p == 0 && t == 6) {
          gather_q(1);
          if (sub == 0 && lane == 0 && ok)
            __hip_atomic_store((GAS unsigned long long*)(pws + WS_EH + h * LINE), ((unsigned long long)qhi << 32) | qlo, __ATOMIC_RELAXED, __HIP_MEMORY_SCOPE_AGENT);
        }
      }
    }
    float f[4];
    {
      const unsigned ts = now();
      if (hw == 0) while (ml_state != 2 && now() - ts <= SPIN_LIMIT) { ml_step(true); if (ml_state != 2) __builtin_amdgcn_s_sleep(16); }
      while (*(volatile LAS unsigned*)(lds + L_SCL + 1) != (unsigned)p + 1u && now() - ts <= SPIN_LIMIT) __builtin_amdgcn_s_sleep(4);
      float m, l;
      stats(f, m, l);
      const float scl = (now() - ts <= SPIN_LIMIT && ok) ? *(volatile LAS float*)(lds + L_SCL) : __builtin_nanf("");
#pragma unroll
      for (int w = 0; w < 4; ++w) f[w] *= scl;
    }
    asm volatile("s_waitcnt vmcnt(0)" ::: "memory");
    u32x4 W[32];
#pragma unroll
    for (int u = 0; u < 16; ++u) {
      const u32x4 r0 = *(LAS const u32x4*)(lds + L_WO + (hw * 32 + 2 * u) * 256 + lane * 4), r1 = *(LAS const u32x4*)(lds + L_WO + (hw * 32 + 2 * u + 1) * 256 + lane * 4);
#pragma unroll
      for (int k = 0; k < 2; ++k) {
        W[2 * u][2 * k] = __builtin_amdgcn_perm(r1[k], r0[k], 0x05040100u);
        W[2 * u][2 * k + 1] = __builtin_amdgcn_perm(r1[k], r0[k], 0x07060302u);
        W[2 * u + 1][2 * k] = __builtin_amdgcn_perm(r1[k + 2], r0[k + 2], 0x05040100u);
        W[2 * u + 1][2 * k + 1] = __builtin_amdgcn_perm(r1[k + 2], r0[k + 2], 0x07060302u);
      }
    }
#pragma unroll
    for (int t = 0; t < 32; ++t) asm volatile("" : "+v"(W[t]));
    LAS const float* op0 = lds + L_OP + p * 512 + 8 * c + 2 * g;
    asm volatile("" : "+v"(op0));
    float y[8];
#pragma unroll
    for (int k = 0; k < 8; ++k) y[k] = 0.f;
    asm volatile("" : "+v"(f[0]), "+v"(f[1]), "+v"(f[2]), "+v"(f[3]), "+v"(y[0]), "+v"(y[1]), "+v"(y[2]), "+v"(y[3]), "+v"(y[4]), "+v"(y[5]), "+v"(y[6]), "+v"(y[7]));
    if (hw == 1) {
      const unsigned ts = now();
      while (*(volatile LAS unsigned*)(lds + L_CTL + 1) != (unsigned)p + 1u && now() - ts <= SPIN_LIMIT) { control(); __builtin_amdgcn_s_sleep(4); }
    }
    if (p) __builtin_amdgcn_s_setprio(3);
    lds_barrier();
    f32x2 o = f[0] * *(LAS const f32x2*)op0;
#pragma unroll
    for (int w = 1; w < 4; ++w) o += f[w] * *(LAS const f32x2*)(op0 + w * 128);
    const unsigned op = pack_bf16(o[0], o[1]);
#define O1(U) { const unsigned ob = dppu<BCAST0 + (U)>(op); _Pragma("unroll") for (int k = 0; k < 4; ++k) { y[k] = dot2(W[2 * (U)][k], ob, y[k]); y[4 + k] = dot2(W[2 * (U) + 1][k], ob, y[4 + k]); } }
    O1(0) O1(1) O1(2) O1(3) O1(4) O1(5) O1(6) O1(7) O1(8) O1(9) O1(10) O1(11) O1(12) O1(13) O1(14) O1(15)
#undef O1
    const f32x2 yy = fold_rows(y);
    ya = yb;
    yb = pair_granule(yy[0], yy[1], p ? ytag1 : ytag0);
    PIN();
    if (p == 0) tw = now();
  }
  __builtin_amdgcn_raw_buffer_store_b128(u32x4{ya[0], ya[1], yb[0], yb[1]}, rw, yoff, 0, SC1);
  if (hw != 0) return;
  {
    u32x4 e[6][4];
    float s0 = 0.f, s1 = 0.f;
    auto fetch = [&](u32x4 (&d)[4]) {
#pragma unroll
      for (int k = 0; k < 4; ++k) d[k] = __builtin_amdgcn_raw_buffer_load_b128(rw, gath, k * GSTR, SC1);
      PIN();
    };
    auto there = [&](const u32x4& d) { return pair_valid(d, ytag0, ytag1); };
    auto take = [&](const u32x4 (&d)[4]) {
      float a0 = 0.f, a1 = 0.f, b0 = 0.f, b1 = 0.f;
#pragma unroll
      for (int k = 0; k < 4; ++k) { a0 += pair_first(d[k][1]); a1 += pair_second(d[k][0], d[k][1]); b0 += pair_first(d[k][3]); b1 += pair_second(d[k][2], d[k][3]); }
      a0 += dpp<XOR2>(a0); a1 += dpp<XOR2>(a1); b0 += dpp<XOR2>(b0); b1 += dpp<XOR2>(b1);
      s0 = (lane & 2) ? b0 : a0;
      s1 = (lane & 2) ? b1 : a1;
      return (bool)__all((int)there(d[0]) & (int)there(d[1]) & (int)there(d[2]) & (int)there(d[3]));
    };
    __builtin_amdgcn_s_sleep(POLL_T0);
    fetch(e[0]); __builtin_amdgcn_s_sleep(POLL_GAP);
    fetch(e[1]); __builtin_amdgcn_s_sleep(POLL_GAP);
    fetch(e[2]);
    bool all = take(e[0]);
    if (!all) { fetch(e[3]); all = take(e[1]);
      if (!all) { fetch(e[4]); all = take(e[2]);
        if (!all) { fetch(e[5]); all = take(e[3]);
          if (!all) { all = take(e[4]);
            if (!all) {
              const unsigned ts = now();
              while (!(all = take(e[5])) && now() - ts <= SPIN_LIMIT) {
                __builtin_amdgcn_s_sleep(4);
#pragma unroll
                for (int k = 0; k < 4; ++k) e[4][k] = __builtin_amdgcn_raw_buffer_load_b128(rw, there(e[5][k]) ? WS_END : gath + k * GSTR, 0, SC1);
#pragma unroll
                for (int k = 0; k < 4; ++k) if (!there(e[5][k])) e[5][k] = e[4][k];
              }
            } } } } }
    s0 += dpp<XOR8>(s0); s1 += dpp<XOR8>(s1);
    s0 += dpp<ROR4>(s0); s1 += dpp<ROR4>(s1);
    s0 = swap16_add(s0, s0); s1 = swap16_add(s1, s1);
    s0 = swap32_add(s0, s0); s1 = swap32_add(s1, s1);
    if (!(ok && all)) { s0 = __builtin_nanf(""); s1 = s0; }
    const auto rout = __builtin_amdgcn_make_buffer_rsrc((void*)pout, 0, 2 * 2 * DM * 2, 0x00020000);
    if (lane < 4) __builtin_amdgcn_raw_buffer_store_b32(pack_bf16(s0, s1), rout, (b * 2 * DM + (lane >> 1) * DM + mem * 4) * 2 + (lane & 1) * 4, 0, 0);
    if (mem == 0 && lane == 0 && all)
      __hip_atomic_store((GAS unsigned long long*)(pws + WS_EG + b * LINE), ((unsigned long long)thi << 32) | tlo, __ATOMIC_RELAXED, __HIP_MEMORY_SCOPE_AGENT);
    PIN();
#pragma unroll
    for (int i = 0; i < 6; ++i) asm volatile("" :: "v"(e[i][0]), "v"(e[i][1]), "v"(e[i][2]), "v"(e[i][3]));
  }
}

extern "C" void kernel_launch(void* const* d_in, const int* in_sizes, int n_in, void* d_out, int out_size, void* d_ws,
                              size_t ws_size, hipStream_t stream) {
  Params P;
  for (int p = 0; p < 2; ++p) {
    P.x[p] = (const unsigned char*)d_in[5 * p + 0];
    P.k[p] = (const unsigned char*)d_in[5 * p + 1];
    P.v[p] = (const unsigned char*)d_in[5 * p + 2];
    P.wq[p] = (const unsigned char*)d_in[5 * p + 3];
    P.wo[p] = (const unsigned char*)d_in[5 * p + 4];
  }
  P.out = (unsigned char*)d_out;
  P.ws = (unsigned char*)d_ws + (ws_size > (size_t)(8u << 20) ? ((ws_size - WS_END - (1u << 20)) & ~(size_t)0x1fffff) : 0);
  mha_decode_kernel<<<NWG, NTHR, 0, stream>>>(P);
}
```

```cpp
#include <hip/hip_runtime.h>
#include <hip/hip_bf16.h>
#include <stdint.h>

typedef unsigned u32x2 __attribute__((ext_vector_type(2)));
typedef unsigned u32x4 __attribute__((ext_vector_type(4)));
typedef unsigned u32x16 __attribute__((ext_vector_type(16)));
typedef float f32x2 __attribute__((ext_vector_type(2)));
typedef float f32x4 __attribute__((ext_vector_type(4)));
typedef __bf16 bf16x2 __attribute__((ext_vector_type(2)));
typedef __bf16 bf16x8 __attribute__((ext_vector_type(8)));
#define LAS __attribute__((address_space(3)))
#define GAS __attribute__((address_space(1)))

constexpr unsigned PACE_DQ = 13760;
constexpr unsigned PACE_DQ1 = 14500;
constexpr unsigned PACE_T0 = 217;
constexpr unsigned Q_DELAY = 200;
constexpr unsigned WO_PARK = 180;
constexpr unsigned WO_T0 = 1700, WO_GAP0 = 52, WO_GAP1 = 80;
constexpr int POLL_T0 = 18, POLL_GAP = 6;
constexpr unsigned AD_LGP = 8;
constexpr int AD_D = 180;
constexpr unsigned AD_T0 = 1100;
constexpr int AD_TI = 3000;
constexpr float AD_CALM = 125.f;
constexpr float AD_TP = 1200.f;
constexpr float AD_RISE = 10.f, AD_STEP = 0.006f, AD_LEAK = 0.0015f, AD_MAX = 0.2f;

constexpr int NH = 32, DH = 128, DM = 512;
constexpr int NWG = 256, NTHR = 512;
constexpr unsigned REGION = 1u << 19;
constexpr int NT = 2, SC1 = 16;
constexpr float C2 = 0.08838834764831845f * 1.4426950408889634f;

constexpr unsigned LINE = 256;
constexpr unsigned WS_EG = 0, WS_EH = WS_EG + 2 * LINE, WS_ML = WS_EH + 32 * LINE, WS_CTL = WS_ML + 2 * NWG * LINE, WS_QX = WS_CTL + 128 * LINE,
                   WS_Y = WS_QX + 32 * 8192, WS_END = WS_Y + NWG * 4096;
constexpr int L_SC = 0;
constexpr int L_OP = L_SC + 2048;
constexpr int L_Q = L_OP + 1024;
constexpr int L_ML = L_Q + 128;
constexpr int L_TAG = L_ML + 16;
constexpr int L_CTL = L_TAG + 2;
constexpr int L_SCL = L_TAG + 4;
constexpr int L_LATE = L_TAG + 8;
constexpr int L_WO = L_LATE + 256;
constexpr int L_END = L_WO + 32768;

struct Params {
  const unsigned char* x[2];
  const unsigned char* k[2];
  const unsigned char* v[2];
  const unsigned char* wq[2];
  const unsigned char* wo[2];
  unsigned char* out;
  unsigned char* ws;
};

__device__ __forceinline__ float dot2(unsigned a, unsigned b, float acc) {
  return __builtin_amdgcn_fdot2_f32_bf16(__builtin_bit_cast(bf16x2, a), __builtin_bit_cast(bf16x2, b), acc, false);
}
template <int CTRL> __device__ __forceinline__ unsigned dppu(unsigned x) { return (unsigned)__builtin_amdgcn_mov_dpp((int)x, CTRL, 0xf, 0xf, true); }
template <int CTRL> __device__ __forceinline__ float dpp(float x) { return __builtin_bit_cast(float, dppu<CTRL>(__builtin_bit_cast(unsigned, x))); }
constexpr int XOR1 = 0xB1, XOR2 = 0x4E, XOR7 = 0x141, XOR8 = 0x128, ROR4 = 0x124, BCAST0 = 0x150;
__device__ __forceinline__ float swap16_add(float a, float b) {
  auto s = __builtin_amdgcn_permlane16_swap(__float_as_uint(a), __float_as_uint(b), false, false);
  return __uint_as_float(s[0]) + __uint_as_float(s[1]);
}
__device__ __forceinline__ float swap32_add(float a, float b) {
  auto s = __builtin_amdgcn_permlane32_swap(__float_as_uint(a), __float_as_uint(b), false, false);
  return __uint_as_float(s[0]) + __uint_as_float(s[1]);
}
__device__ __forceinline__ float xrow_max(float x) {
  auto s = __builtin_amdgcn_permlane16_swap(__float_as_uint(x), __float_as_uint(x), false, false);
  x = fmaxf(__uint_as_float(s[0]), __uint_as_float(s[1]));
  auto t = __builtin_amdgcn_permlane32_swap(__float_as_uint(x), __float_as_uint(x), false, false);
  return fmaxf(__uint_as_float(t[0]), __uint_as_float(t[1]));
}
__device__ __forceinline__ f32x2 fold_rows(const float (&a)[8]) {
  const float z0 = swap32_add(a[0], a[4]), z1 = swap32_add(a[1], a[5]), z2 = swap32_add(a[2], a[6]), z3 = swap32_add(a[3], a[7]);
  return f32x2{swap16_add(z0, z2), swap16_add(z1, z3)};
}
__device__ __forceinline__ unsigned pack_bf16(float lo, float hi) {
  bf16x2 r;
  r[0] = (__bf16)lo;
  r[1] = (__bf16)hi;
  return __builtin_bit_cast(unsigned, r);
}
template <int T> __device__ __forceinline__ u32x4 diag_of(unsigned w, const u32x4& diag) {
  return u32x4{dppu<BCAST0 + T>(w) & diag[0], dppu<BCAST0 + T>(w) & diag[1], dppu<BCAST0 + T>(w) & diag[2], dppu<BCAST0 + T>(w) & diag[3]};
}
__device__ __forceinline__ f32x4 rows_mac(f32x4 acc, const u32x4& a, const u32x4& v) {
  return __builtin_amdgcn_mfma_f32_16x16x32_bf16(__builtin_bit_cast(bf16x8, a), __builtin_bit_cast(bf16x8, v), acc, 0, 0, 0);
}
__device__ __forceinline__ u32x4 diag_mask(int c) {
  const unsigned half = (c & 1) ? 0xffff0000u : 0x0000ffffu;
  return u32x4{(c >> 1) == 0 ? half : 0u, (c >> 1) == 1 ? half : 0u, (c >> 1) == 2 ? half : 0u, (c >> 1) == 3 ? half : 0u};
}
__device__ __forceinline__ float round24(float v) { return __uint_as_float((__float_as_uint(v) + 0x80u) & 0xffffff00u); }
__device__ __forceinline__ u32x4 granules(float v0, float v1, unsigned tlo, unsigned thi) {
  return u32x4{tlo, __float_as_uint(round24(v0)) | thi, tlo, __float_as_uint(round24(v1)) | thi};
}
__device__ __forceinline__ bool valid(const u32x4& e, unsigned tlo, unsigned thi) {
  return (((e[0] ^ tlo) | (e[2] ^ tlo)) | (((e[1] ^ thi) | (e[3] ^ thi)) & 0xffu)) == 0u;
}
__device__ __forceinline__ float val(unsigned u) { return __uint_as_float(u & 0xffffff00u); }
__device__ __forceinline__ u32x2 pair_granule(float v0, float v1, unsigned tag24) {
  const unsigned r0 = (__float_as_uint(v0) + 0x800u) & 0xfffff000u, r1 = __float_as_uint(v1) + 0x800u;
  return u32x2{((r1 << 12) & 0xff000000u) | tag24, r0 | (r1 >> 20)};
}
__device__ __forceinline__ float pair_first(unsigned hi) { return __uint_as_float(hi & 0xfffff000u); }
__device__ __forceinline__ float pair_second(unsigned lo, unsigned hi) { return __uint_as_float((hi << 20) | ((lo >> 12) & 0xff000u)); }
__device__ __forceinline__ unsigned pair_tag0(unsigned tlo) { return tlo & 0xffffffu; }
__device__ __forceinline__ unsigned pair_tag1(unsigned tlo, unsigned thi) { return (5u * tlo + ((tlo >> 24) | (thi << 8)) + 1u) & 0xffffffu; }
__device__ __forceinline__ bool pair_valid(const u32x4& d, unsigned tag0, unsigned tag1) { return (((d[0] ^ tag0) | (d[2] ^ tag1)) & 0xffffffu) == 0u; }
template <int N> __device__ __forceinline__ void wait_vm() { __builtin_amdgcn_s_waitcnt((N & 15) | ((N >> 4) << 14) | (7 << 4) | (15 << 8)); }
__device__ __forceinline__ void wait_lgkm0() { __builtin_amdgcn_s_waitcnt(0xC07F); }
__device__ __forceinline__ unsigned now() { return (unsigned)__builtin_amdgcn_s_memrealtime(); }
__device__ __forceinline__ u32x2 sload2(const unsigned char* p) {
  u32x2 e;
  asm volatile("s_load_dwordx2 %0, %1, 0x0\n\ts_waitcnt lgkmcnt(0)" : "=&s"(e) : "s"(p) : "memory");
  return e;
}
__device__ __forceinline__ u32x16 sload16(const unsigned char* p) {
  u32x16 e;
  asm volatile("s_load_dwordx16 %0, %1, 0x0\n\ts_waitcnt lgkmcnt(0)" : "=&s"(e) : "s"(p) : "memory");
  return e;
}
#define PIN() __builtin_amdgcn_sched_barrier(0)
__device__ __forceinline__ void lds_barrier() {
  asm volatile("" ::: "memory");
  wait_lgkm0();
  __builtin_amdgcn_s_barrier();
  asm volatile("" ::: "memory");
  PIN();
}
constexpr unsigned SPIN_LIMIT = 5000000u;

__global__ void __launch_bounds__(NTHR, 2) mha_decode_kernel(Params P) {
  __shared__ __attribute__((aligned(16))) float lds_raw[L_END];
  LAS float* lds = (LAS float*)lds_raw;
  const unsigned t0 = now();
  PIN();
  const int bid = blockIdx.x;
  const int h = (bid & 7) * 4 + (bid >> 6), sub = (bid >> 3) & 7;
  const int b = sub >> 2, qd = sub & 3;
  const int lane = threadIdx.x & 63;
  const int wave = __builtin_amdgcn_readfirstlane(threadIdx.x >> 6);
  const int g = lane >> 4, c = lane & 15;
  const unsigned mem = 4 * h + qd;

  const size_t rofs = (size_t)(b * 128 + mem) * REGION;
  const unsigned char *px = b ? P.x[1] : P.x[0], *pwq = b ? P.wq[1] : P.wq[0];
  const unsigned char *pk0 = P.k[0] + rofs, *pk1 = P.k[1] + rofs, *pv0 = P.v[0] + rofs, *pv1 = P.v[1] + rofs, *pwo0 = P.wo[0], *pwo1 = P.wo[1];
  unsigned char *pout = P.out, *pws = P.ws;
  asm volatile("" : "+s"(px), "+s"(pwq), "+s"(pk0), "+s"(pk1), "+s"(pv0), "+s"(pv1), "+s"(pwo0), "+s"(pwo1), "+s"(pout), "+s"(pws));

  const unsigned voff = lane * 16 + ((wave & 1) << 13) + ((wave & 2) << 14);
  unsigned pos = 2 * h + (qd >> 1) + 4 * (qd & 1);
  unsigned tq = ((t0 + PACE_T0) << 8) + b * (PACE_DQ / 2);
  unsigned dq = PACE_DQ1;
  u32x4 R[12][4];
  auto issue = [&](u32x4 (&dst)[4], const __amdgpu_buffer_rsrc_t& r) {
    const unsigned s = ((pos & 4u) << 10) | ((pos & 1u) << 14) | ((pos & 2u) << 15) | ((pos & 24u) << 14);
#pragma unroll
    for (int t = 0; t < 4; ++t) dst[t] = __builtin_amdgcn_raw_buffer_load_b128(r, voff + t * 1024, s, NT);
    pos += 1;
    tq += dq;
    PIN();
  };
  auto rk = __builtin_amdgcn_make_buffer_rsrc((void*)pk0, 0, (int)REGION, 0x00020000);
  if (wave < 4) issue(R[0], rk);
  int role = wave;
  asm volatile("" : "+s"(role));

  const auto rw = __builtin_amdgcn_make_buffer_rsrc((void*)pws, 0, (int)WS_END, 0x00020000);
  bool ok = true;
  unsigned qlo = 0, qhi = 0;
  auto gather_q = [&](int p) {
    const unsigned ts = now(), tag0 = pair_tag0(qlo), tag1 = pair_tag1(qlo, qhi);
    const unsigned src = WS_QX + h * 8192 + (4 * p + (lane >> 5)) * 1024 + b * 512 + (lane & 31) * 16;
    u32x4 e0, e1;
    bool all;
    do {
      e0 = __builtin_amdgcn_raw_buffer_load_b128(rw, src, 0, SC1);
      e1 = __builtin_amdgcn_raw_buffer_load_b128(rw, src, 2048, SC1);
      all = __all((int)pair_valid(e0, tag0, tag1) & (int)pair_valid(e1, tag0, tag1));
      if (!all) __builtin_amdgcn_s_sleep(16);
    } while (!all && now() - ts <= SPIN_LIMIT);
    ok = ok && all;
    float v0 = pair_first(e0[1]) + pair_first(e1[1]), v1 = pair_second(e0[0], e0[1]) + pair_second(e1[0], e1[1]);
    float v2 = pair_first(e0[3]) + pair_first(e1[3]), v3 = pair_second(e0[2], e0[3]) + pair_second(e1[2], e1[3]);
    v0 = swap32_add(v0, v0); v1 = swap32_add(v1, v1); v2 = swap32_add(v2, v2); v3 = swap32_add(v3, v3);
    if (lane < 32) *(LAS u32x2*)(lds + L_Q + p * 64 + 4 * c + 2 * g) = u32x2{pack_bf16(v0, v1), pack_bf16(v2, v3)};
  };
  auto park_wo = [&](const unsigned char* pw) {
    const auto r = __builtin_amdgcn_make_buffer_rsrc((void*)(pw + h * 131072 + sub * 16384), 0, 16384, 0x00020000);
    const unsigned d0 = __builtin_amdgcn_raw_buffer_load_b32(r, lane * 128, 0, NT);
    PIN();
    const unsigned e0 = __builtin_amdgcn_raw_buffer_load_b32(r, lane * 128 + 64, 0, SC1);
    PIN();
    const unsigned d1 = __builtin_amdgcn_raw_buffer_load_b32(r, lane * 128, 8192, NT);
    PIN();
    const unsigned e1 = __builtin_amdgcn_raw_buffer_load_b32(r, lane * 128 + 64, 8192, SC1);
    PIN();
    asm volatile("" :: "v"(d0), "v"(e0), "v"(d1), "v"(e1));
  };
  if (role == 4) {
    const auto rq = __builtin_amdgcn_make_buffer_rsrc((void*)(pwq + (size_t)qd * 128 * (NH * DH * 2) + h * (DH * 2)), 0, 128 * NH * DH * 2 - h * (DH * 2), 0x00020000);
    const auto rx = __builtin_amdgcn_make_buffer_rsrc((void*)px, 0, 2 * DM * 2, 0x00020000);
    const unsigned xo = (128 * qd + 4 * c + g) * 2;
    const unsigned x00 = __builtin_amdgcn_raw_buffer_load_b16(rx, xo, 0, NT), x01 = __builtin_amdgcn_raw_buffer_load_b16(rx, xo + DM * 2, 0, NT);
    const unsigned x10 = __builtin_amdgcn_raw_buffer_load_b16(rx, xo + 128, 0, NT), x11 = __builtin_amdgcn_raw_buffer_load_b16(rx, xo + 128 + DM * 2, 0, NT);
    u32x4 W[32];
    PIN();
    const unsigned vo = g * (NH * DH * 2) + c * 16;
#pragma unroll
    for (int t = 0; t < 32; ++t) W[t] = __builtin_amdgcn_raw_buffer_load_b128(rq, vo, t * 4 * (NH * DH * 2), NT);
    PIN();
    const u32x2 prev = sload2(pws + WS_EH + h * LINE);
    qlo = prev[0] + 1u;
    qhi = (prev[1] + (qlo == 0u ? 1u : 0u)) & 0xffu;
    if (lane < 4) *(volatile LAS unsigned*)(lds + L_ML + 4 * lane + 2) = 0u;
    const unsigned xa = x00 | (x01 << 16), xb = x10 | (x11 << 16);
    unsigned sel[4];
#pragma unroll
    for (int k = 0; k < 4; ++k) {
      const unsigned pick = (c & 8) ? 0x0302u : 0x0100u;
      sel[k] = ((c & 7) == 2 * k ? pick : 0x0c0cu) | (((c & 7) == 2 * k + 1 ? pick : 0x0c0cu) << 16);
    }
    f32x4 d0 = {0.f, 0.f, 0.f, 0.f}, d1 = {0.f, 0.f, 0.f, 0.f};
#define QM(T, X, D) { const unsigned xt = dppu<BCAST0 + ((T) & 15)>(X); \
      const u32x4 a = {__builtin_amdgcn_perm(xt, xt, sel[0]), __builtin_amdgcn_perm(xt, xt, sel[1]), __builtin_amdgcn_perm(xt, xt, sel[2]), __builtin_amdgcn_perm(xt, xt, sel[3])}; \
      D = rows_mac(D, a, W[T]); }
#define QM4(T, X) QM(T, X, d0) QM(T + 1, X, d1) QM(T + 2, X, d0) QM(T + 3, X, d1) PIN();
    QM4(0, xa) QM4(4, xa) QM4(8, xa) QM4(12, xa) QM4(16, xb) QM4(20, xb) QM4(24, xb) QM4(28, xb)
#undef QM4
#undef QM
    d0 += d1;
    const u32x2 g0 = pair_granule(d0[0], d0[1], pair_tag0(qlo)), g1 = pair_granule(d0[2], d0[3], pair_tag1(qlo, qhi));
    __builtin_amdgcn_raw_buffer_store_b128(u32x4{g0[0], g0[1], g1[0], g1[1]}, rw, WS_QX + h * 8192 + sub * 1024 + lane * 16, 0, SC1);
    const unsigned ts = now();
    while (now() - ts < Q_DELAY) __builtin_amdgcn_s_sleep(1);
    gather_q(0);
  } else if (role == 5) {
    const u32x2 prev = sload2(pws + WS_EG + b * LINE);
    const unsigned tlo = prev[0] + 1u, thi = (prev[1] + (tlo == 0u ? 1u : 0u)) & 0xffu;
    if (lane == 0) { *(LAS u32x4*)(lds + L_TAG) = u32x4{tlo, thi, PACE_DQ, 0u}; *(LAS u32x4*)(lds + L_SCL) = u32x4{0u, 0u, 0u, 0u}; }
    *(LAS u32x4*)(lds + L_LATE + 4 * lane) = u32x4{0u, 0u, 0u, 0u};
  } else if (role == 7) {
    while (now() - t0 < WO_PARK) __builtin_amdgcn_s_sleep(1);
    park_wo(pwo0);
    park_wo(pwo1);
  }
  asm volatile("" : "+s"(role));

  if (role < 4) {
    __builtin_amdgcn_s_setprio(3);
    auto gate = [&]() {
      PIN();
      wait_vm<4>();
      while ((int)((now() << 8) - tq) < 0) __builtin_amdgcn_s_sleep(1);
      PIN();
    };
    auto gate_ctl = [&](bool slow, bool report) {
      PIN();
      wait_vm<4>();
      const unsigned ctl = __builtin_amdgcn_readfirstlane(*(volatile LAS unsigned*)(lds + L_CTL));
      if (report) *(volatile LAS int*)(lds + L_LATE + wave * 64 + lane) = (int)((now() << 8) - tq + 2 * dq);
      dq = (slow && ctl < PACE_DQ1) ? PACE_DQ1 : ctl;
      while ((int)((now() << 8) - tq) < 0) __builtin_amdgcn_s_sleep(1);
      PIN();
    };
#pragma unroll
    for (int j = 1; j < 12; ++j) { gate(); issue(R[j], rk); }
    lds_barrier();
    u32x4 q = *(LAS const u32x4*)(lds + L_Q + 4 * c);
    LAS float* sc = lds + L_SC + wave * 512 + lane;
    float keep = 0.f, mx = -INFINITY;
    auto score = [&](const u32x4 (&K)[4], int J, int row) {
      float x[4];
#pragma unroll
      for (int t = 0; t < 4; ++t) {
        float d = dot2(K[t][0], q[0], 0.f);
        d = dot2(K[t][1], q[1], d);
        d = dot2(K[t][2], q[2], d);
        x[t] = dot2(K[t][3], q[3], d);
      }
      const float a0 = x[0] + dpp<XOR8>(x[0]), a1 = x[1] + dpp<XOR8>(x[1]), a2 = x[2] + dpp<XOR8>(x[2]), a3 = x[3] + dpp<XOR8>(x[3]);
      const float y0 = (c & 8) ? a2 : a0, y1 = (c & 8) ? a3 : a1;
      const float b0 = y0 + dpp<XOR7>(y0), b1 = y1 + dpp<XOR7>(y1);
      float s = (c & 4) ? b1 : b0;
      s += dpp<XOR2>(s);
      s += dpp<XOR1>(s);
      keep = ((c & 3) == J) ? s : keep;
      if (J == 3) { sc[row * 64] = keep; mx = fmaxf(mx, keep); }
      asm volatile("" : "+v"(keep), "+v"(mx));
    };
    score(R[0], 0, 0); score(R[1], 1, 0); score(R[2], 2, 0); score(R[3], 3, 0);
    gate_ctl(true, true); issue(R[0], rk);
    score(R[4], 0, 1); score(R[5], 1, 1); score(R[6], 2, 1); score(R[7], 3, 1);
    gate(); issue(R[1], rk);
    score(R[8], 0, 2); score(R[9], 1, 2); score(R[10], 2, 2);
    gate(); issue(R[2], rk);
    score(R[11], 3, 2); score(R[0], 0, 3);
    gate(); issue(R[3], rk);
    score(R[1], 1, 3);
    const u32x4 diag = diag_mask(c);
    int it = 4, slow_end = 5;
#pragma unroll 1
    for (int p = 0; p < 2; ++p) {
      const auto rv = __builtin_amdgcn_make_buffer_rsrc((void*)(p ? pv1 : pv0), 0, (int)REGION, 0x00020000);
#pragma unroll 1
      for (; it < 8; ++it) {
#pragma unroll
        for (int j = 0; j < 4; ++j) {
          if ((j & 1) == 0) gate_ctl(it < slow_end, j == 0); else gate();
          issue(R[j], rk);
          score(R[(j + 2) & 3], (j + 2) & 3, j < 2 ? it - 1 : it);
        }
      }
      gate(); issue(R[0], rv); score(R[2], 2, 7);
      gate(); issue(R[1], rv); score(R[3], 3, 7);
      mx = fmaxf(mx, dpp<XOR1>(mx)); mx = fmaxf(mx, dpp<XOR2>(mx)); mx = fmaxf(mx, dpp<XOR7>(mx)); mx = fmaxf(mx, dpp<XOR8>(mx));
      mx = xrow_max(mx);
      const float m2 = mx * C2;
      {
        float l = 0.f;
#pragma unroll
        for (int u = 0; u < 8; ++u) {
          const float e = __builtin_amdgcn_exp2f(fmaf(sc[u * 64], C2, -m2));
          sc[u * 64] = __uint_as_float(pack_bf16(e, e));
          l += e;
        }
        l += dpp<XOR1>(l); l += dpp<XOR2>(l); l += dpp<XOR7>(l); l += dpp<XOR8>(l);
        l = swap16_add(l, l);
        l = swap32_add(l, l);
        if (lane == 0) {
          *(LAS f32x2*)(lds + L_ML + 4 * wave) = f32x2{m2, l};
          wait_lgkm0();
          *(volatile LAS unsigned*)(lds + L_ML + 4 * wave + 2) = p + 1;
        }
      }
      f32x4 acc = {0.f, 0.f, 0.f, 0.f};
      u32x4 a0, a1, a2, a3;
#define PVA(J) do { a0 = diag_of<0 + J>(pr, diag); a1 = diag_of<4 + J>(pr, diag); a2 = diag_of<8 + J>(pr, diag); a3 = diag_of<12 + J>(pr, diag); \
    asm volatile("" : "+v"(a0), "+v"(a1), "+v"(a2), "+v"(a3)); } while (0)
#define PVB(RR) do { acc = rows_mac(acc, a0, RR[0]); acc = rows_mac(acc, a1, RR[1]); acc = rows_mac(acc, a2, RR[2]); acc = rows_mac(acc, a3, RR[3]); \
    asm volatile("" : "+v"(acc)); } while (0)
      unsigned pr = __float_as_uint(sc[0]);
#pragma unroll 1
      for (int u = 0; u < 7; ++u) {
        const unsigned pn = __float_as_uint(sc[(u + 1) * 64]);
        if (u == 6 && p && wave == 0 && lane == 0) *(volatile LAS unsigned*)(lds + L_CTL + 1) = 2u;
        PVA(0); gate_ctl(false, true); issue(R[2], rv); PVB(R[0]);
        PVA(1); gate(); issue(R[3], rv); PVB(R[1]);
        PVA(2); gate_ctl(false, false); issue(R[0], rv); PVB(R[2]);
        PVA(3); gate(); issue(R[1], rv); PVB(R[3]);
        pr = pn;
      }
      rk = __builtin_amdgcn_make_buffer_rsrc((void*)pk1, 0, p ? 0 : (int)REGION, 0x00020000);
      auto gate_end = [&]() {
        PIN();
        wait_vm<4>();
        if (p == 0) while ((int)((now() << 8) - tq) < 0) __builtin_amdgcn_s_sleep(1);
        PIN();
      };
      PVA(0); gate(); issue(R[2], rv); PVB(R[0]);
      PVA(1); gate(); issue(R[3], rv); PVB(R[1]);
      if (wave == 0 && lane == 0) *(volatile LAS unsigned*)(lds + L_CTL + 1) = p + 1;
      PVA(2); gate_end(); issue(R[0], rk); PVB(R[2]);
      PVA(3); gate_end(); issue(R[1], rk); PVB(R[3]);
#undef PVA
#undef PVB
      if (lane < 32) *(LAS f32x4*)(lds + L_OP + p * 512 + wave * 128 + 8 * c + 4 * g) = acc;
      lds_barrier();
      if (p) break;
      q = *(LAS const u32x4*)(lds + L_Q + 64 + 4 * c);
      mx = -INFINITY;
      gate(); issue(R[2], rk); score(R[0], 0, 0);
      gate(); issue(R[3], rk); score(R[1], 1, 0);
      it = 1;
      slow_end = 0;
    }
    return;
  }

  const int hw = wave - 4;
  lds_barrier();
  const u32x2 tg2 = *(LAS const u32x2*)(lds + L_TAG);
  const unsigned tlo = __builtin_amdgcn_readfirstlane(tg2[0]), thi = __builtin_amdgcn_readfirstlane(tg2[1]);
  unsigned yoff = WS_Y + (b * 128 + mem) * 4096 + hw * 1024 + c * 64 + g * 16;
  unsigned gath = WS_Y + (b * 128 + (lane >> 1)) * 4096 + mem * 32 + (lane & 1) * 16;
  asm volatile("" : "+v"(yoff), "+v"(gath));
  constexpr unsigned GSTR = 32 * 4096;
  auto stats_ready = [&](int p) { return (bool)__all(*(volatile LAS unsigned*)(lds + L_ML + 4 * (lane & 3) + 2) == (unsigned)p + 1u); };
  auto stats = [&](float (&f)[4], float& m, float& l) {
    asm volatile("" ::: "memory");
    float mw[4], lw[4];
#pragma unroll
    for (int w = 0; w < 4; ++w) { const f32x2 ml = *(volatile LAS f32x2*)(lds + L_ML + 4 * w); mw[w] = ml[0]; lw[w] = ml[1]; }
    m = round24(fmaxf(fmaxf(mw[0], mw[1]), fmaxf(mw[2], mw[3])));
    l = 0.f;
#pragma unroll
    for (int w = 0; w < 4; ++w) { f[w] = __builtin_amdgcn_exp2f(mw[w] - m); l = fmaf(f[w], lw[w], l); }
  };
  const unsigned ytag0 = pair_tag0(tlo), ytag1 = pair_tag1(tlo, thi);
  unsigned tr = ((t0 + AD_T0) >> AD_LGP) << AD_LGP, tc = tr;
  float c_int = 0.f, c_prev = 0.f;
  auto control = [&]() {
    if ((bid & 0xd8) == 0 && (int)(now() - tr) >= 0) {
      float a = (float)*(volatile LAS int*)(lds + L_LATE + (lane & 3) * 64);
      a += dpp<XOR1>(a); a += dpp<XOR2>(a);
      const u32x4 gr = granules(a * (0.25f / 256.f), 0.f, tlo, thi);
      if (lane == 0) __builtin_amdgcn_raw_buffer_store_b64(u32x2{gr[0], gr[1]}, rw, WS_CTL + (((tr >> AD_LGP) & 63u) * 2 + b) * LINE + (bid & 7) * 8, 0, SC1);
      tr = ((now() >> AD_LGP) + 1u) << AD_LGP;
    }
    if ((int)(now() - tc) >= AD_D) {
      const u32x16 e = sload16(pws + WS_CTL + (((tc >> AD_LGP) & 63u) * 2 + b) * LINE);
      float n = 0.f, hi = 0.f, lo = 0.f;
#pragma unroll
      for (int k = 0; k < 8; ++k) {
        const bool v0 = e[2 * k] == tlo && (e[2 * k + 1] & 0xffu) == thi;
        const float l0 = v0 ? val(e[2 * k + 1]) : 0.f;
        n += v0 ? 1.f : 0.f;
        lo = fmaxf(lo, fminf(hi, l0));
        hi = fmaxf(hi, l0);
      }
      const float err = 0.5f * (hi + lo);
      const float pterm = fmaxf(err - AD_CALM, 0.f) * (1.f / AD_TP);
      if (n >= 5.f) {
        if (err < AD_CALM) c_int = fmaxf(c_int - AD_LEAK, 0.f);
        else if ((int)(tc - t0) > AD_TI && err > c_prev + AD_RISE) c_int += AD_STEP;
        c_prev = err;
        if (lane == 0) *(volatile LAS unsigned*)(lds + L_CTL) = (unsigned)((float)PACE_DQ * (1.f + fminf(c_int + pterm, AD_MAX)));
      }
      tc += 1u << AD_LGP;
    }
  };
  unsigned tw = t0 + WO_T0;
  u32x2 ya = {0u, 0u}, yb = {0u, 0u};
#pragma unroll 1
  for (int p = 0; p < 2; ++p) {
    const auto ro = __builtin_amdgcn_make_buffer_rsrc((void*)(p ? pwo1 : pwo0), 0, DM * NH * DH * 2, 0x00020000);
    const unsigned wvo = (unsigned)(h * DH + 2 * g) * (DM * 2) + hw * 256 + c * 16;
    const unsigned gap = p ? WO_GAP1 : WO_GAP0;
    tw += (h * gap) >> 5;
    int ml_state = 0;
    unsigned ml_time = 0;
    auto ml_step = [&](bool last) {
      if (ml_state == 0 && stats_ready(p)) {
        float f[4], m, l;
        stats(f, m, l);
        if (lane == 0) __builtin_amdgcn_raw_buffer_store_b128(granules(m, l, tlo, thi), rw, WS_ML + (p * NWG + bid) * LINE, 0, SC1);
        ml_state = 1;
        ml_time = now();
      } else if (ml_state == 1 && (last || now() - ml_time > 400u)) {
        const u32x4 e = __builtin_amdgcn_raw_buffer_load_b128(rw, WS_ML + (p * NWG + (bid ^ ((lane & 3) << 3))) * LINE, 0, SC1);
        ml_time = now();
        if (__all(valid(e, tlo, thi))) {
          const float m0 = val(e[1]);
          float M = fmaxf(m0, dpp<XOR1>(m0));
          M = fmaxf(M, dpp<XOR2>(M));
          const float e0 = __builtin_amdgcn_exp2f(m0 - M);
          float L = e0 * val(e[3]);
          L += dpp<XOR1>(L);
          L += dpp<XOR2>(L);
          if (lane == 0) {
            *(volatile LAS float*)(lds + L_SCL) = e0 / L;
            wait_lgkm0();
            *(volatile LAS unsigned*)(lds + L_SCL + 1) = p + 1;
          }
          ml_state = 2;
        }
      }
    };
#pragma unroll 1
    for (int t = 0; t < 32; ++t) {
      while ((int)(now() - tw) < 0) {
        if (hw == 1) control();
        __builtin_amdgcn_s_sleep(4);
      }
      tw += gap;
      __builtin_amdgcn_raw_ptr_buffer_load_lds(ro, (LAS void*)(lds + L_WO + (hw * 32 + t) * 256), 16, wvo, ((t >> 1) * 8 + (t & 1)) * (DM * 2), 0, NT);
      if (hw == 0) {
        ml_step(false);
        if (p == 0 && t == 6) {
          gather_q(1);
          if (sub == 0 && lane == 0 && ok)
            __hip_atomic_store((GAS unsigned long long*)(pws + WS_EH + h * LINE), ((unsigned long long)qhi << 32) | qlo, __ATOMIC_RELAXED, __HIP_MEMORY_SCOPE_AGENT);
        }
      }
    }
    float f[4];
    {
      const unsigned ts = now();
      if (hw == 0) while (ml_state != 2 && now() - ts <= SPIN_LIMIT) { ml_step(true); if (ml_state != 2) __builtin_amdgcn_s_sleep(16); }
      while (*(volatile LAS unsigned*)(lds + L_SCL + 1) != (unsigned)p + 1u && now() - ts <= SPIN_LIMIT) __builtin_amdgcn_s_sleep(4);
      float m, l;
      stats(f, m, l);
      const float scl = (now() - ts <= SPIN_LIMIT && ok) ? *(volatile LAS float*)(lds + L_SCL) : __builtin_nanf("");
#pragma unroll
      for (int w = 0; w < 4; ++w) f[w] *= scl;
    }
    asm volatile("s_waitcnt vmcnt(0)" ::: "memory");
    u32x4 W[32];
#pragma unroll
    for (int u = 0; u < 16; ++u) {
      const u32x4 r0 = *(LAS const u32x4*)(lds + L_WO + (hw * 32 + 2 * u) * 256 + lane * 4), r1 = *(LAS const u32x4*)(lds + L_WO + (hw * 32 + 2 * u + 1) * 256 + lane * 4);
#pragma unroll
      for (int k = 0; k < 2; ++k) {
        W[2 * u][2 * k] = __builtin_amdgcn_perm(r1[k], r0[k], 0x05040100u);
        W[2 * u][2 * k + 1] = __builtin_amdgcn_perm(r1[k], r0[k], 0x07060302u);
        W[2 * u + 1][2 * k] = __builtin_amdgcn_perm(r1[k + 2], r0[k + 2], 0x05040100u);
        W[2 * u + 1][2 * k + 1] = __builtin_amdgcn_perm(r1[k + 2], r0[k + 2], 0x07060302u);
      }
    }
#pragma unroll
    for (int t = 0; t < 32; ++t) asm volatile("" : "+v"(W[t]));
    LAS const float* op0 = lds + L_OP + p * 512 + 8 * c + 2 * g;
    asm volatile("" : "+v"(op0));
    float y[8];
#pragma unroll
    for (int k = 0; k < 8; ++k) y[k] = 0.f;
    asm volatile("" : "+v"(f[0]), "+v"(f[1]), "+v"(f[2]), "+v"(f[3]), "+v"(y[0]), "+v"(y[1]), "+v"(y[2]), "+v"(y[3]), "+v"(y[4]), "+v"(y[5]), "+v"(y[6]), "+v"(y[7]));
    if (hw == 1) {
      const unsigned ts = now();
      while (*(volatile LAS unsigned*)(lds + L_CTL + 1) != (unsigned)p + 1u && now() - ts <= SPIN_LIMIT) { control(); __builtin_amdgcn_s_sleep(4); }
    }
    if (p) __builtin_amdgcn_s_setprio(3);
    lds_barrier();
    f32x2 o = f[0] * *(LAS const f32x2*)op0;
#pragma unroll
    for (int w = 1; w < 4; ++w) o += f[w] * *(LAS const f32x2*)(op0 + w * 128);
    const unsigned op = pack_bf16(o[0], o[1]);
#define O1(U) { const unsigned ob = dppu<BCAST0 + (U)>(op); _Pragma("unroll") for (int k = 0; k < 4; ++k) { y[k] = dot2(W[2 * (U)][k], ob, y[k]); y[4 + k] = dot2(W[2 * (U) + 1][k], ob, y[4 + k]); } }
    O1(0) O1(1) O1(2) O1(3) O1(4) O1(5) O1(6) O1(7) O1(8) O1(9) O1(10) O1(11) O1(12) O1(13) O1(14) O1(15)
#undef O1
    const f32x2 yy = fold_rows(y);
    ya = yb;
    yb = pair_granule(yy[0], yy[1], p ? ytag1 : ytag0);
    PIN();
    if (p == 0) tw = now();
  }
  __builtin_amdgcn_raw_buffer_store_b128(u32x4{ya[0], ya[1], yb[0], yb[1]}, rw, yoff, 0, SC1);
  if (hw != 0) return;
  {
    u32x4 e[6][4];
    float s0 = 0.f, s1 = 0.f;
    auto fetch = [&](u32x4 (&d)[4]) {
#pragma unroll
      for (int k = 0; k < 4; ++k) d[k] = __builtin_amdgcn_raw_buffer_load_b128(rw, gath, k * GSTR, SC1);
      PIN();
    };
    auto there = [&](const u32x4& d) { return pair_valid(d, ytag0, ytag1); };
    auto take = [&](const u32x4 (&d)[4]) {
      float a0 = 0.f, a1 = 0.f, b0 = 0.f, b1 = 0.f;
#pragma unroll
      for (int k = 0; k < 4; ++k) { a0 += pair_first(d[k][1]); a1 += pair_second(d[k][0], d[k][1]); b0 += pair_first(d[k][3]); b1 += pair_second(d[k][2], d[k][3]); }
      a0 += dpp<XOR2>(a0); a1 += dpp<XOR2>(a1); b0 += dpp<XOR2>(b0); b1 += dpp<XOR2>(b1);
      s0 = (lane & 2) ? b0 : a0;
      s1 = (lane & 2) ? b1 : a1;
      return (bool)__all((int)there(d[0]) & (int)there(d[1]) & (int)there(d[2]) & (int)there(d[3]));
    };
    __builtin_amdgcn_s_sleep(POLL_T0);
    fetch(e[0]); __builtin_amdgcn_s_sleep(POLL_GAP);
    fetch(e[1]); __builtin_amdgcn_s_sleep(POLL_GAP);
    fetch(e[2]);
    bool all = take(e[0]);
    if (!all) { fetch(e[3]); all = take(e[1]);
      if (!all) { fetch(e[4]); all = take(e[2]);
        if (!all) { fetch(e[5]); all = take(e[3]);
          if (!all) { all = take(e[4]);
            if (!all) {
              const unsigned ts = now();
              while (!(all = take(e[5])) && now() - ts <= SPIN_LIMIT) {
                __builtin_amdgcn_s_sleep(4);
#pragma unroll
                for (int k = 0; k < 4; ++k) e[4][k] = __builtin_amdgcn_raw_buffer_load_b128(rw, there(e[5][k]) ? WS_END : gath + k * GSTR, 0, SC1);
#pragma unroll
                for (int k = 0; k < 4; ++k) if (!there(e[5][k])) e[5][k] = e[4][k];
              }
            } } } } }
    s0 += dpp<XOR8>(s0); s1 += dpp<XOR8>(s1);
    s0 += dpp<ROR4>(s0); s1 += dpp<ROR4>(s1);
    s0 = swap16_add(s0, s0); s1 = swap16_add(s1, s1);
    s0 = swap32_add(s0, s0); s1 = swap32_add(s1, s1);
    if (!(ok && all)) { s0 = __builtin_nanf(""); s1 = s0; }
    const auto rout = __builtin_amdgcn_make_buffer_rsrc((void*)pout, 0, 2 * 2 * DM * 2, 0x00020000);
    if (lane < 4) __builtin_amdgcn_raw_buffer_store_b32(pack_bf16(s0, s1), rout, (b * 2 * DM + (lane >> 1) * DM + mem * 4) * 2 + (lane & 1) * 4, 0, 0);
    if (mem == 0 && lane == 0 && all)
      __hip_atomic_store((GAS unsigned long long*)(pws + WS_EG + b * LINE), ((unsigned long long)thi << 32) | tlo, __ATOMIC_RELAXED, __HIP_MEMORY_SCOPE_AGENT);
    PIN();
#pragma unroll
    for (int i = 0; i < 6; ++i) asm volatile("" :: "v"(e[i][0]), "v"(e[i][1]), "v"(e[i][2]), "v"(e[i][3]));
  }
}

extern "C" void kernel_launch(void* const* d_in, const int* in_sizes, int n_in, void* d_out, int out_size, void* d_ws,
                              size_t ws_size, hipStream_t stream) {
  Params P;
  for (int p = 0; p < 2; ++p) {
    P.x[p] = (const unsigned char*)d_in[5 * p + 0];
    P.k[p] = (const unsigned char*)d_in[5 * p + 1];
    P.v[p] = (const unsigned char*)d_in[5 * p + 2];
    P.wq[p] = (const unsigned char*)d_in[5 * p + 3];
    P.wo[p] = (const unsigned char*)d_in[5 * p + 4];
  }
  P.out = (unsigned char*)d_out;
  P.ws = (unsigned char*)d_ws + (ws_size > (size_t)(8u << 20) ? ((ws_size - WS_END - (1u << 20)) & ~(size_t)0x1fffff) : 0);
  mha_decode_kernel<<<NWG, NTHR, 0, stream>>>(P);
}
```

```cpp
#include <hip/hip_runtime.h>
#include <hip/hip_bf16.h>
#include <stdint.h>

typedef unsigned u32x2 __attribute__((ext_vector_type(2)));
typedef unsigned u32x4 __attribute__((ext_vector_type(4)));
typedef unsigned u32x16 __attribute__((ext_vector_type(16)));
typedef float f32x2 __attribute__((ext_vector_type(2)));
typedef float f32x4 __attribute__((ext_vector_type(4)));
typedef __bf16 bf16x2 __attribute__((ext_vector_type(2)));
typedef __bf16 bf16x8 __attribute__((ext_vector_type(8)));
#define LAS __attribute__((address_space(3)))
#define GAS __attribute__((address_space(1)))

constexpr unsigned PACE_DQ = 13760;
constexpr unsigned PACE_DQ1 = 14500;
constexpr unsigned PACE_T0 = 217;
constexpr unsigned Q_DELAY = 200;
constexpr unsigned WO_PARK = 180;
constexpr unsigned WO_T0 = 1700, WO_GAP0 = 52, WO_GAP1 = 80;
constexpr int POLL_T0 = 22, POLL_GAP = 8;
constexpr unsigned AD_LGP = 8;
constexpr int AD_D = 180;
constexpr unsigned AD_T0 = 1100;
constexpr int AD_TI = 3000;
constexpr float AD_CALM = 125.f;
constexpr float AD_TP = 1200.f;
constexpr float AD_RISE = 10.f, AD_STEP = 0.006f, AD_LEAK = 0.0015f, AD_MAX = 0.2f;

constexpr int NH = 32, DH = 128, DM = 512;
constexpr int NWG = 256, NTHR = 512;
constexpr unsigned REGION = 1u << 19;
constexpr int NT = 2, SC1 = 16;
constexpr float C2 = 0.08838834764831845f * 1.4426950408889634f;

constexpr unsigned LINE = 256;
constexpr unsigned WS_EG = 0, WS_EH = WS_EG + 2 * LINE, WS_ML = WS_EH + 32 * LINE, WS_CTL = WS_ML + 2 * NWG * LINE, WS_QX = WS_CTL + 128 * LINE,
                   WS_Y = WS_QX + 32 * 8192, WS_END = WS_Y + NWG * 4096;
constexpr int L_SC = 0;
constexpr int L_OP = L_SC + 2048;
constexpr int L_Q = L_OP + 1024;
constexpr int L_ML = L_Q + 128;
constexpr int L_TAG = L_ML + 16;
constexpr int L_CTL = L_TAG + 2;
constexpr int L_SCL = L_TAG + 4;
constexpr int L_LATE = L_TAG + 8;
constexpr int L_WO = L_LATE + 256;
constexpr int L_END = L_WO + 32768;

struct Params {
  const unsigned char* x[2];
  const unsigned char* k[2];
  const unsigned char* v[2];
  const unsigned char* wq[2];
  const unsigned char* wo[2];
  unsigned char* out;
  unsigned char* ws;
};

__device__ __forceinline__ float dot2(unsigned a, unsigned b, float acc) {
  return __builtin_amdgcn_fdot2_f32_bf16(__builtin_bit_cast(bf16x2, a), __builtin_bit_cast(bf16x2, b), acc, false);
}
template <int CTRL> __device__ __forceinline__ unsigned dppu(unsigned x) { return (unsigned)__builtin_amdgcn_mov_dpp((int)x, CTRL, 0xf, 0xf, true); }
template <int CTRL> __device__ __forceinline__ float dpp(float x) { return __builtin_bit_cast(float, dppu<CTRL>(__builtin_bit_cast(unsigned, x))); }
constexpr int XOR1 = 0xB1, XOR2 = 0x4E, XOR7 = 0x141, XOR8 = 0x128, ROR4 = 0x124, BCAST0 = 0x150;
__device__ __forceinline__ float swap16_add(float a, float b) {
  auto s = __builtin_amdgcn_permlane16_swap(__float_as_uint(a), __float_as_uint(b), false, false);
  return __uint_as_float(s[0]) + __uint_as_float(s[1]);
}
__device__ __forceinline__ float swap32_add(float a, float b) {
  auto s = __builtin_amdgcn_permlane32_swap(__float_as_uint(a), __float_as_uint(b), false, false);
  return __uint_as_float(s[0]) + __uint_as_float(s[1]);
}
__device__ __forceinline__ float xrow_max(float x) {
  auto s = __builtin_amdgcn_permlane16_swap(__float_as_uint(x), __float_as_uint(x), false, false);
  x = fmaxf(__uint_as_float(s[0]), __uint_as_float(s[1]));
  auto t = __builtin_amdgcn_permlane32_swap(__float_as_uint(x), __float_as_uint(x), false, false);
  return fmaxf(__uint_as_float(t[0]), __uint_as_float(t[1]));
}
__device__ __forceinline__ f32x2 fold_rows(const float (&a)[8]) {
  const float z0 = swap32_add(a[0], a[4]), z1 = swap32_add(a[1], a[5]), z2 = swap32_add(a[2], a[6]), z3 = swap32_add(a[3], a[7]);
  return f32x2{swap16_add(z0, z2), swap16_add(z1, z3)};
}
__device__ __forceinline__ unsigned pack_bf16(float lo, float hi) {
  bf16x2 r;
  r[0] = (__bf16)lo;
  r[1] = (__bf16)hi;
  return __builtin_bit_cast(unsigned, r);
}
template <int T> __device__ __forceinline__ u32x4 diag_of(unsigned w, const u32x4& diag) {
  return u32x4{dppu<BCAST0 + T>(w) & diag[0], dppu<BCAST0 + T>(w) & diag[1], dppu<BCAST0 + T>(w) & diag[2], dppu<BCAST0 + T>(w) & diag[3]};
}
__device__ __forceinline__ f32x4 rows_mac(f32x4 acc, const u32x4& a, const u32x4& v) {
  return __builtin_amdgcn_mfma_f32_16x16x32_bf16(__builtin_bit_cast(bf16x8, a), __builtin_bit_cast(bf16x8, v), acc, 0, 0, 0);
}
__device__ __forceinline__ u32x4 diag_mask(int c) {
  const unsigned half = (c & 1) ? 0xffff0000u : 0x0000ffffu;
  return u32x4{(c >> 1) == 0 ? half : 0u, (c >> 1) == 1 ? half : 0u, (c >> 1) == 2 ? half : 0u, (c >> 1) == 3 ? half : 0u};
}
__device__ __forceinline__ float round24(float v) { return __uint_as_float((__float_as_uint(v) + 0x80u) & 0xffffff00u); }
__device__ __forceinline__ u32x4 granules(float v0, float v1, unsigned tlo, unsigned thi) {
  return u32x4{tlo, __float_as_uint(round24(v0)) | thi, tlo, __float_as_uint(round24(v1)) | thi};
}
__device__ __forceinline__ bool valid(const u32x4& e, unsigned tlo, unsigned thi) {
  return (((e[0] ^ tlo) | (e[2] ^ tlo)) | (((e[1] ^ thi) | (e[3] ^ thi)) & 0xffu)) == 0u;
}
__device__ __forceinline__ float val(unsigned u) { return __uint_as_float(u & 0xffffff00u); }
__device__ __forceinline__ u32x2 pair_granule(float v0, float v1, unsigned tag24) {
  const unsigned r0 = (__float_as_uint(v0) + 0x800u) & 0xfffff000u, r1 = __float_as_uint(v1) + 0x800u;
  return u32x2{((r1 << 12) & 0xff000000u) | tag24, r0 | (r1 >> 20)};
}
__device__ __forceinline__ float pair_first(unsigned hi) { return __uint_as_float(hi & 0xfffff000u); }
__device__ __forceinline__ float pair_second(unsigned lo, unsigned hi) { return __uint_as_float((hi << 20) | ((lo >> 12) & 0xff000u)); }
__device__ __forceinline__ unsigned pair_tag0(unsigned tlo) { return tlo & 0xffffffu; }
__device__ __forceinline__ unsigned pair_tag1(unsigned tlo, unsigned thi) { return (5u * tlo + ((tlo >> 24) | (thi << 8)) + 1u) & 0xffffffu; }
__device__ __forceinline__ bool pair_valid(const u32x4& d, unsigned tag0, unsigned tag1) { return (((d[0] ^ tag0) | (d[2] ^ tag1)) & 0xffffffu) == 0u; }
template <int N> __device__ __forceinline__ void wait_vm() { __builtin_amdgcn_s_waitcnt((N & 15) | ((N >> 4) << 14) | (7 << 4) | (15 << 8)); }
__device__ __forceinline__ void wait_lgkm0() { __builtin_amdgcn_s_waitcnt(0xC07F); }
__device__ __forceinline__ unsigned now() { return (unsigned)__builtin_amdgcn_s_memrealtime(); }
__device__ __forceinline__ u32x2 sload2(const unsigned char* p) {
  u32x2 e;
  asm volatile("s_load_dwordx2 %0, %1, 0x0\n\ts_waitcnt lgkmcnt(0)" : "=&s"(e) : "s"(p) : "memory");
  return e;
}
__device__ __forceinline__ u32x16 sload16(const unsigned char* p) {
  u32x16 e;
  asm volatile("s_load_dwordx16 %0, %1, 0x0\n\ts_waitcnt lgkmcnt(0)" : "=&s"(e) : "s"(p) : "memory");
  return e;
}
#define PIN() __builtin_amdgcn_sched_barrier(0)
__device__ __forceinline__ void lds_barrier() {
  asm volatile("" ::: "memory");
  wait_lgkm0();
  __builtin_amdgcn_s_barrier();
  asm volatile("" ::: "memory");
  PIN();
}
constexpr unsigned SPIN_LIMIT = 5000000u;

__global__ void __launch_bounds__(NTHR, 2) mha_decode_kernel(Params P) {
  __shared__ __attribute__((aligned(16))) float lds_raw[L_END];
  LAS float* lds = (LAS float*)lds_raw;
  const unsigned t0 = now();
  PIN();
  const int bid = blockIdx.x;
  const int h = (bid & 7) * 4 + (bid >> 6), sub = (bid >> 3) & 7;
  const int b = sub >> 2, qd = sub & 3;
  const int lane = threadIdx.x & 63;
  const int wave = __builtin_amdgcn_readfirstlane(threadIdx.x >> 6);
  const int g = lane >> 4, c = lane & 15;
  const unsigned mem = 4 * h + qd;

  const size_t rofs = (size_t)(b * 128 + mem) * REGION;
  const unsigned char *px = b ? P.x[1] : P.x[0], *pwq = b ? P.wq[1] : P.wq[0];
  const unsigned char *pk0 = P.k[0] + rofs, *pk1 = P.k[1] + rofs, *pv0 = P.v[0] + rofs, *pv1 = P.v[1] + rofs, *pwo0 = P.wo[0], *pwo1 = P.wo[1];
  unsigned char *pout = P.out, *pws = P.ws;
  asm volatile("" : "+s"(px), "+s"(pwq), "+s"(pk0), "+s"(pk1), "+s"(pv0), "+s"(pv1), "+s"(pwo0), "+s"(pwo1), "+s"(pout), "+s"(pws));

  const unsigned voff = lane * 16 + ((wave & 1) << 13) + ((wave & 2) << 14);
  unsigned pos = 2 * h + (qd >> 1) + 4 * (qd & 1);
  unsigned tq = ((t0 + PACE_T0) << 8) + b * (PACE_DQ / 2);
  unsigned dq = PACE_DQ1;
  u32x4 R[12][4];
  auto issue = [&](u32x4 (&dst)[4], const __amdgpu_buffer_rsrc_t& r) {
    const unsigned s = ((pos & 4u) << 10) | ((pos & 1u) << 14) | ((pos & 2u) << 15) | ((pos & 24u) << 14);
#pragma unroll
    for (int t = 0; t < 4; ++t) dst[t] = __builtin_amdgcn_raw_buffer_load_b128(r, voff + t * 1024, s, NT);
    pos += 1;
    tq += dq;
    PIN();
  };
  auto rk = __builtin_amdgcn_make_buffer_rsrc((void*)pk0, 0, (int)REGION, 0x00020000);
  if (wave < 4) issue(R[0], rk);
  int role = wave;
  asm volatile("" : "+s"(role));

  const auto rw = __builtin_amdgcn_make_buffer_rsrc((void*)pws, 0, (int)WS_END, 0x00020000);
  bool ok = true;
  unsigned qlo = 0, qhi = 0;
  auto gather_q = [&](int p) {
    const unsigned ts = now(), tag0 = pair_tag0(qlo), tag1 = pair_tag1(qlo, qhi);
    const unsigned src = WS_QX + h * 8192 + (4 * p + (lane >> 5)) * 1024 + b * 512 + (lane & 31) * 16;
    u32x4 e0, e1;
    bool all;
    do {
      e0 = __builtin_amdgcn_raw_buffer_load_b128(rw, src, 0, SC1);
      e1 = __builtin_amdgcn_raw_buffer_load_b128(rw, src, 2048, SC1);
      all = __all((int)pair_valid(e0, tag0, tag1) & (int)pair_valid(e1, tag0, tag1));
      if (!all) __builtin_amdgcn_s_sleep(16);
    } while (!all && now() - ts <= SPIN_LIMIT);
    ok = ok && all;
    float v0 = pair_first(e0[1]) + pair_first(e1[1]), v1 = pair_second(e0[0], e0[1]) + pair_second(e1[0], e1[1]);
    float v2 = pair_first(e0[3]) + pair_first(e1[3]), v3 = pair_second(e0[2], e0[3]) + pair_second(e1[2], e1[3]);
    v0 = swap32_add(v0, v0); v1 = swap32_add(v1, v1); v2 = swap32_add(v2, v2); v3 = swap32_add(v3, v3);
    if (lane < 32) *(LAS u32x2*)(lds + L_Q + p * 64 + 4 * c + 2 * g) = u32x2{pack_bf16(v0, v1), pack_bf16(v2, v3)};
  };
  auto park_wo = [&](const unsigned char* pw) {
    const auto r = __builtin_amdgcn_make_buffer_rsrc((void*)(pw + h * 131072 + sub * 16384), 0, 16384, 0x00020000);
    const unsigned d0 = __builtin_amdgcn_raw_buffer_load_b32(r, lane * 128, 0, NT);
    PIN();
    const unsigned e0 = __builtin_amdgcn_raw_buffer_load_b32(r, lane * 128 + 64, 0, SC1);
    PIN();
    const unsigned d1 = __builtin_amdgcn_raw_buffer_load_b32(r, lane * 128, 8192, NT);
    PIN();
    const unsigned e1 = __builtin_amdgcn_raw_buffer_load_b32(r, lane * 128 + 64, 8192, SC1);
    PIN();
    asm volatile("" :: "v"(d0), "v"(e0), "v"(d1), "v"(e1));
  };
  if (role == 4) {
    const auto rq = __builtin_amdgcn_make_buffer_rsrc((void*)(pwq + (size_t)qd * 128 * (NH * DH * 2) + h * (DH * 2)), 0, 128 * NH * DH * 2 - h * (DH * 2), 0x00020000);
    const auto rx = __builtin_amdgcn_make_buffer_rsrc((void*)px, 0, 2 * DM * 2, 0x00020000);
    const unsigned xo = (128 * qd + 4 * c + g) * 2;
    const unsigned x00 = __builtin_amdgcn_raw_buffer_load_b16(rx, xo, 0, NT), x01 = __builtin_amdgcn_raw_buffer_load_b16(rx, xo + DM * 2, 0, NT);
    const unsigned x10 = __builtin_amdgcn_raw_buffer_load_b16(rx, xo + 128, 0, NT), x11 = __builtin_amdgcn_raw_buffer_load_b16(rx, xo + 128 + DM * 2, 0, NT);
    u32x4 W[32];
    PIN();
    const unsigned vo = g * (NH * DH * 2) + c * 16;
#pragma unroll
    for (int t = 0; t < 32; ++t) W[t] = __builtin_amdgcn_raw_buffer_load_b128(rq, vo, t * 4 * (NH * DH * 2), NT);
    PIN();
    const u32x2 prev = sload2(pws + WS_EH + h * LINE);
    qlo = prev[0] + 1u;
    qhi = (prev[1] + (qlo == 0u ? 1u : 0u)) & 0xffu;
    if (lane < 4) *(volatile LAS unsigned*)(lds + L_ML + 4 * lane + 2) = 0u;
    const unsigned xa = x00 | (x01 << 16), xb = x10 | (x11 << 16);
    unsigned sel[4];
#pragma unroll
    for (int k = 0; k < 4; ++k) {
      const unsigned pick = (c & 8) ? 0x0302u : 0x0100u;
      sel[k] = ((c & 7) == 2 * k ? pick : 0x0c0cu) | (((c & 7) == 2 * k + 1 ? pick : 0x0c0cu) << 16);
    }
    f32x4 d0 = {0.f, 0.f, 0.f, 0.f}, d1 = {0.f, 0.f, 0.f, 0.f};
#define QM(T, X, D) { const unsigned xt = dppu<BCAST0 + ((T) & 15)>(X); \
      const u32x4 a = {__builtin_amdgcn_perm(xt, xt, sel[0]), __builtin_amdgcn_perm(xt, xt, sel[1]), __builtin_amdgcn_perm(xt, xt, sel[2]), __builtin_amdgcn_perm(xt, xt, sel[3])}; \
      D = rows_mac(D, a, W[T]); }
#define QM4(T, X) QM(T, X, d0) QM(T + 1, X, d1) QM(T + 2, X, d0) QM(T + 3, X, d1) PIN();
    QM4(0, xa) QM4(4, xa) QM4(8, xa) QM4(12, xa) QM4(16, xb) QM4(20, xb) QM4(24, xb) QM4(28, xb)
#undef QM4
#undef QM
    d0 += d1;
    const u32x2 g0 = pair_granule(d0[0], d0[1], pair_tag0(qlo)), g1 = pair_granule(d0[2], d0[3], pair_tag1(qlo, qhi));
    __builtin_amdgcn_raw_buffer_store_b128(u32x4{g0[0], g0[1], g1[0], g1[1]}, rw, WS_QX + h * 8192 + sub * 1024 + lane * 16, 0, SC1);
    const unsigned ts = now();
    while (now() - ts < Q_DELAY) __builtin_amdgcn_s_sleep(1);
    gather_q(0);
  } else if (role == 5) {
    const u32x2 prev = sload2(pws + WS_EG + b * LINE);
    const unsigned tlo = prev[0] + 1u, thi = (prev[1] + (tlo == 0u ? 1u : 0u)) & 0xffu;
    if (lane == 0) { *(LAS u32x4*)(lds + L_TAG) = u32x4{tlo, thi, PACE_DQ, 0u}; *(LAS u32x4*)(lds + L_SCL) = u32x4{0u, 0u, 0u, 0u}; }
    *(LAS u32x4*)(lds + L_LATE + 4 * lane) = u32x4{0u, 0u, 0u, 0u};
  } else if (role == 7) {
    while (now() - t0 < WO_PARK) __builtin_amdgcn_s_sleep(1);
    park_wo(pwo0);
    park_wo(pwo1);
  }
  asm volatile("" : "+s"(role));

  if (role < 4) {
    __builtin_amdgcn_s_setprio(3);
    auto gate = [&]() {
      PIN();
      wait_vm<4>();
      while ((int)((now() << 8) - tq) < 0) __builtin_amdgcn_s_sleep(1);
      PIN();
    };
    auto gate_ctl = [&](bool slow, bool report) {
      PIN();
      wait_vm<4>();
      const unsigned ctl = __builtin_amdgcn_readfirstlane(*(volatile LAS unsigned*)(lds + L_CTL));
      if (report) *(volatile LAS int*)(lds + L_LATE + wave * 64 + lane) = (int)((now() << 8) - tq + 2 * dq);
      dq = (slow && ctl < PACE_DQ1) ? PACE_DQ1 : ctl;
      while ((int)((now() << 8) - tq) < 0) __builtin_amdgcn_s_sleep(1);
      PIN();
    };
#pragma unroll
    for (int j = 1; j < 12; ++j) { gate(); issue(R[j], rk); }
    lds_barrier();
    u32x4 q = *(LAS const u32x4*)(lds + L_Q + 4 * c);
    LAS float* sc = lds + L_SC + wave * 512 + lane;
    float keep = 0.f, mx = -INFINITY;
    auto score = [&](const u32x4 (&K)[4], int J, int row) {
      float x[4];
#pragma unroll
      for (int t = 0; t < 4; ++t) {
        float d = dot2(K[t][0], q[0], 0.f);
        d = dot2(K[t][1], q[1], d);
        d = dot2(K[t][2], q[2], d);
        x[t] = dot2(K[t][3], q[3], d);
      }
      const float a0 = x[0] + dpp<XOR8>(x[0]), a1 = x[1] + dpp<XOR8>(x[1]), a2 = x[2] + dpp<XOR8>(x[2]), a3 = x[3] + dpp<XOR8>(x[3]);
      const float y0 = (c & 8) ? a2 : a0, y1 = (c & 8) ? a3 : a1;
      const float b0 = y0 + dpp<XOR7>(y0), b1 = y1 + dpp<XOR7>(y1);
      float s = (c & 4) ? b1 : b0;
      s += dpp<XOR2>(s);
      s += dpp<XOR1>(s);
      keep = ((c & 3) == J) ? s : keep;
      if (J == 3) { sc[row * 64] = keep; mx = fmaxf(mx, keep); }
      asm volatile("" : "+v"(keep), "+v"(mx));
    };
    score(R[0], 0, 0); score(R[1], 1, 0); score(R[2], 2, 0); score(R[3], 3, 0);
    gate_ctl(true, true); issue(R[0], rk);
    score(R[4], 0, 1); score(R[5], 1, 1); score(R[6], 2, 1); score(R[7], 3, 1);
    gate(); issue(R[1], rk);
    score(R[8], 0, 2); score(R[9], 1, 2); score(R[10], 2, 2);
    gate(); issue(R[2], rk);
    score(R[11], 3, 2); score(R[0], 0, 3);
    gate(); issue(R[3], rk);
    score(R[1], 1, 3);
    const u32x4 diag = diag_mask(c);
    int it = 4, slow_end = 5;
#pragma unroll 1
    for (int p = 0; p < 2; ++p) {
      const auto rv = __builtin_amdgcn_make_buffer_rsrc((void*)(p ? pv1 : pv0), 0, (int)REGION, 0x00020000);
#pragma unroll 1
      for (; it < 8; ++it) {
#pragma unroll
        for (int j = 0; j < 4; ++j) {
          if ((j & 1) == 0) gate_ctl(it < slow_end, j == 0); else gate();
          issue(R[j], rk);
          score(R[(j + 2) & 3], (j + 2) & 3, j < 2 ? it - 1 : it);
        }
      }
      gate(); issue(R[0], rv); score(R[2], 2, 7);
      gate(); issue(R[1], rv); score(R[3], 3, 7);
      mx = fmaxf(mx, dpp<XOR1>(mx)); mx = fmaxf(mx, dpp<XOR2>(mx)); mx = fmaxf(mx, dpp<XOR7>(mx)); mx = fmaxf(mx, dpp<XOR8>(mx));
      mx = xrow_max(mx);
      const float m2 = mx * C2;
      {
        float l = 0.f;
#pragma unroll
        for (int u = 0; u < 8; ++u) {
          const float e = __builtin_amdgcn_exp2f(fmaf(sc[u * 64], C2, -m2));
          sc[u * 64] = __uint_as_float(pack_bf16(e, e));
          l += e;
        }
        l += dpp<XOR1>(l); l += dpp<XOR2>(l); l += dpp<XOR7>(l); l += dpp<XOR8>(l);
        l = swap16_add(l, l);
        l = swap32_add(l, l);
        if (lane == 0) {
          *(LAS f32x2*)(lds + L_ML + 4 * wave) = f32x2{m2, l};
          wait_lgkm0();
          *(volatile LAS unsigned*)(lds + L_ML + 4 * wave + 2) = p + 1;
        }
      }
      f32x4 acc = {0.f, 0.f, 0.f, 0.f};
      u32x4 a0, a1, a2, a3;
#define PVA(J) do { a0 = diag_of<0 + J>(pr, diag); a1 = diag_of<4 + J>(pr, diag); a2 = diag_of<8 + J>(pr, diag); a3 = diag_of<12 + J>(pr, diag); \
    asm volatile("" : "+v"(a0), "+v"(a1), "+v"(a2), "+v"(a3)); } while (0)
#define PVB(RR) do { acc = rows_mac(acc, a0, RR[0]); acc = rows_mac(acc, a1, RR[1]); acc = rows_mac(acc, a2, RR[2]); acc = rows_mac(acc, a3, RR[3]); \
    asm volatile("" : "+v"(acc)); } while (0)
      unsigned pr = __float_as_uint(sc[0]);
#pragma unroll 1
      for (int u = 0; u < 7; ++u) {
        const unsigned pn = __float_as_uint(sc[(u + 1) * 64]);
        if (u == 6 && p && wave == 0 && lane == 0) *(volatile LAS unsigned*)(lds + L_CTL + 1) = 2u;
        PVA(0); gate_ctl(false, true); issue(R[2], rv); PVB(R[0]);
        PVA(1); gate(); issue(R[3], rv); PVB(R[1]);
        PVA(2); gate_ctl(false, false); issue(R[0], rv); PVB(R[2]);
        PVA(3); gate(); issue(R[1], rv); PVB(R[3]);
        pr = pn;
      }
      rk = __builtin_amdgcn_make_buffer_rsrc((void*)pk1, 0, p ? 0 : (int)REGION, 0x00020000);
      auto gate_end = [&]() {
        PIN();
        wait_vm<4>();
        if (p == 0) while ((int)((now() << 8) - tq) < 0) __builtin_amdgcn_s_sleep(1);
        PIN();
      };
      PVA(0); gate(); issue(R[2], rv); PVB(R[0]);
      PVA(1); gate(); issue(R[3], rv); PVB(R[1]);
      if (wave == 0 && lane == 0) *(volatile LAS unsigned*)(lds + L_CTL + 1) = p + 1;
      PVA(2); gate_end(); issue(R[0], rk); PVB(R[2]);
      PVA(3); gate_end(); issue(R[1], rk); PVB(R[3]);
#undef PVA
#undef PVB
      if (lane < 32) *(LAS f32x4*)(lds + L_OP + p * 512 + wave * 128 + 8 * c + 4 * g) = acc;
      lds_barrier();
      if (p) break;
      q = *(LAS const u32x4*)(lds + L_Q + 64 + 4 * c);
      mx = -INFINITY;
      gate(); issue(R[2], rk); score(R[0], 0, 0);
      gate(); issue(R[3], rk); score(R[1], 1, 0);
      it = 1;
      slow_end = 0;
    }
    return;
  }

  const int hw = wave - 4;
  lds_barrier();
  const u32x2 tg2 = *(LAS const u32x2*)(lds + L_TAG);
  const unsigned tlo = __builtin_amdgcn_readfirstlane(tg2[0]), thi = __builtin_amdgcn_readfirstlane(tg2[1]);
  unsigned yoff = WS_Y + (b * 128 + mem) * 4096 + hw * 1024 + c * 64 + g * 16;
  unsigned gath = WS_Y + (b * 128 + (lane >> 1)) * 4096 + mem * 32 + (lane & 1) * 16;
  asm volatile("" : "+v"(yoff), "+v"(gath));
  constexpr unsigned GSTR = 32 * 4096;
  auto stats_ready = [&](int p) { return (bool)__all(*(volatile LAS unsigned*)(lds + L_ML + 4 * (lane & 3) + 2) == (unsigned)p + 1u); };
  auto stats = [&](float (&f)[4], float& m, float& l) {
    asm volatile("" ::: "memory");
    float mw[4], lw[4];
#pragma unroll
    for (int w = 0; w < 4; ++w) { const f32x2 ml = *(volatile LAS f32x2*)(lds + L_ML + 4 * w); mw[w] = ml[0]; lw[w] = ml[1]; }
    m = round24(fmaxf(fmaxf(mw[0], mw[1]), fmaxf(mw[2], mw[3])));
    l = 0.f;
#pragma unroll
    for (int w = 0; w < 4; ++w) { f[w] = __builtin_amdgcn_exp2f(mw[w] - m); l = fmaf(f[w], lw[w], l); }
  };
  const unsigned ytag0 = pair_tag0(tlo), ytag1 = pair_tag1(tlo, thi);
  unsigned tr = ((t0 + AD_T0) >> AD_LGP) << AD_LGP, tc = tr;
  float c_int = 0.f, c_prev = 0.f;
  auto control = [&]() {
    if ((bid & 0xd8) == 0 && (int)(now() - tr) >= 0) {
      float a = (float)*(volatile LAS int*)(lds + L_LATE + (lane & 3) * 64);
      a += dpp<XOR1>(a); a += dpp<XOR2>(a);
      const u32x4 gr = granules(a * (0.25f / 256.f), 0.f, tlo, thi);
      if (lane == 0) __builtin_amdgcn_raw_buffer_store_b64(u32x2{gr[0], gr[1]}, rw, WS_CTL + (((tr >> AD_LGP) & 63u) * 2 + b) * LINE + (bid & 7) * 8, 0, SC1);
      tr = ((now() >> AD_LGP) + 1u) << AD_LGP;
    }
    if ((int)(now() - tc) >= AD_D) {
      const u32x16 e = sload16(pws + WS_CTL + (((tc >> AD_LGP) & 63u) * 2 + b) * LINE);
      float n = 0.f, hi = 0.f, lo = 0.f;
#pragma unroll
      for (int k = 0; k < 8; ++k) {
        const bool v0 = e[2 * k] == tlo && (e[2 * k + 1] & 0xffu) == thi;
        const float l0 = v0 ? val(e[2 * k + 1]) : 0.f;
        n += v0 ? 1.f : 0.f;
        lo = fmaxf(lo, fminf(hi, l0));
        hi = fmaxf(hi, l0);
      }
      const float err = 0.5f * (hi + lo);
      const float pterm = fmaxf(err - AD_CALM, 0.f) * (1.f / AD_TP);
      if (n >= 5.f) {
        if (err < AD_CALM) c_int = fmaxf(c_int - AD_LEAK, 0.f);
        else if ((int)(tc - t0) > AD_TI && err > c_prev + AD_RISE) c_int += AD_STEP;
        c_prev = err;
        if (lane == 0) *(volatile LAS unsigned*)(lds + L_CTL) = (unsigned)((float)PACE_DQ * (1.f + fminf(c_int + pterm, AD_MAX)));
      }
      tc += 1u << AD_LGP;
    }
  };
  unsigned tw = t0 + WO_T0;
  u32x2 ya = {0u, 0u}, yb = {0u, 0u};
#pragma unroll 1
  for (int p = 0; p < 2; ++p) {
    const auto ro = __builtin_amdgcn_make_buffer_rsrc((void*)(p ? pwo1 : pwo0), 0, DM * NH * DH * 2, 0x00020000);
    const unsigned wvo = (unsigned)(h * DH + 2 * g) * (DM * 2) + hw * 256 + c * 16;
    const unsigned gap = p ? WO_GAP1 : WO_GAP0;
    tw += (h * gap) >> 5;
    int ml_state = 0;
    unsigned ml_time = 0;
    auto ml_step = [&](bool last) {
      if (ml_state == 0 && stats_ready(p)) {
        float f[4], m, l;
        stats(f, m, l);
        if (lane == 0) __builtin_amdgcn_raw_buffer_store_b128(granules(m, l, tlo, thi), rw, WS_ML + (p * NWG + bid) * LINE, 0, SC1);
        ml_state = 1;
        ml_time = now();
      } else if (ml_state == 1 && (last || now() - ml_time > 400u)) {
        const u32x4 e = __builtin_amdgcn_raw_buffer_load_b128(rw, WS_ML + (p * NWG + (bid ^ ((lane & 3) << 3))) * LINE, 0, SC1);
        ml_time = now();
        if (__all(valid(e, tlo, thi))) {
          const float m0 = val(e[1]);
          float M = fmaxf(m0, dpp<XOR1>(m0));
          M = fmaxf(M, dpp<XOR2>(M));
          const float e0 = __builtin_amdgcn_exp2f(m0 - M);
          float L = e0 * val(e[3]);
          L += dpp<XOR1>(L);
          L += dpp<XOR2>(L);
          if (lane == 0) {
            *(volatile LAS float*)(lds + L_SCL) = e0 / L;
            wait_lgkm0();
            *(volatile LAS unsigned*)(lds + L_SCL + 1) = p + 1;
          }
          ml_state = 2;
        }
      }
    };
#pragma unroll 1
    for (int t = 0; t < 32; ++t) {
      while ((int)(now() - tw) < 0) {
        if (hw == 1) control();
        __builtin_amdgcn_s_sleep(4);
      }
      tw += gap;
      __builtin_amdgcn_raw_ptr_buffer_load_lds(ro, (LAS void*)(lds + L_WO + (hw * 32 + t) * 256), 16, wvo, ((t >> 1) * 8 + (t & 1)) * (DM * 2), 0, NT);
      if (hw == 0) {
        ml_step(false);
        if (p == 0 && t == 6) {
          gather_q(1);
          if (sub == 0 && lane == 0 && ok)
            __hip_atomic_store((GAS unsigned long long*)(pws + WS_EH + h * LINE), ((unsigned long long)qhi << 32) | qlo, __ATOMIC_RELAXED, __HIP_MEMORY_SCOPE_AGENT);
        }
      }
    }
    float f[4];
    {
      const unsigned ts = now();
      if (hw == 0) while (ml_state != 2 && now() - ts <= SPIN_LIMIT) { ml_step(true); if (ml_state != 2) __builtin_amdgcn_s_sleep(16); }
      while (*(volatile LAS unsigned*)(lds + L_SCL + 1) != (unsigned)p + 1u && now() - ts <= SPIN_LIMIT) __builtin_amdgcn_s_sleep(4);
      float m, l;
      stats(f, m, l);
      const float scl = (now() - ts <= SPIN_LIMIT && ok) ? *(volatile LAS float*)(lds + L_SCL) : __builtin_nanf("");
#pragma unroll
      for (int w = 0; w < 4; ++w) f[w] *= scl;
    }
    asm volatile("s_waitcnt vmcnt(0)" ::: "memory");
    u32x4 W[32];
#pragma unroll
    for (int u = 0; u < 16; ++u) {
      const u32x4 r0 = *(LAS const u32x4*)(lds + L_WO + (hw * 32 + 2 * u) * 256 + lane * 4), r1 = *(LAS const u32x4*)(lds + L_WO + (hw * 32 + 2 * u + 1) * 256 + lane * 4);
#pragma unroll
      for (int k = 0; k < 2; ++k) {
        W[2 * u][2 * k] = __builtin_amdgcn_perm(r1[k], r0[k], 0x05040100u);
        W[2 * u][2 * k + 1] = __builtin_amdgcn_perm(r1[k], r0[k], 0x07060302u);
        W[2 * u + 1][2 * k] = __builtin_amdgcn_perm(r1[k + 2], r0[k + 2], 0x05040100u);
        W[2 * u + 1][2 * k + 1] = __builtin_amdgcn_perm(r1[k + 2], r0[k + 2], 0x07060302u);
      }
    }
#pragma unroll
    for (int t = 0; t < 32; ++t) asm volatile("" : "+v"(W[t]));
    LAS const float* op0 = lds + L_OP + p * 512 + 8 * c + 2 * g;
    asm volatile("" : "+v"(op0));
    float y[8];
#pragma unroll
    for (int k = 0; k < 8; ++k) y[k] = 0.f;
    asm volatile("" : "+v"(f[0]), "+v"(f[1]), "+v"(f[2]), "+v"(f[3]), "+v"(y[0]), "+v"(y[1]), "+v"(y[2]), "+v"(y[3]), "+v"(y[4]), "+v"(y[5]), "+v"(y[6]), "+v"(y[7]));
    if (hw == 1) {
      const unsigned ts = now();
      while (*(volatile LAS unsigned*)(lds + L_CTL + 1) != (unsigned)p + 1u && now() - ts <= SPIN_LIMIT) { control(); __builtin_amdgcn_s_sleep(4); }
    }
    if (p) __builtin_amdgcn_s_setprio(3);
    lds_barrier();
    f32x2 o = f[0] * *(LAS const f32x2*)op0;
#pragma unroll
    for (int w = 1; w < 4; ++w) o += f[w] * *(LAS const f32x2*)(op0 + w * 128);
    const unsigned op = pack_bf16(o[0], o[1]);
#define O1(U) { const unsigned ob = dppu<BCAST0 + (U)>(op); _Pragma("unroll") for (int k = 0; k < 4; ++k) { y[k] = dot2(W[2 * (U)][k], ob, y[k]); y[4 + k] = dot2(W[2 * (U) + 1][k], ob, y[4 + k]); } }
    O1(0) O1(1) O1(2) O1(3) O1(4) O1(5) O1(6) O1(7) O1(8) O1(9) O1(10) O1(11) O1(12) O1(13) O1(14) O1(15)
#undef O1
    const f32x2 yy = fold_rows(y);
    ya = yb;
    yb = pair_granule(yy[0], yy[1], p ? ytag1 : ytag0);
    PIN();
    if (p == 0) tw = now();
  }
  __builtin_amdgcn_raw_buffer_store_b128(u32x4{ya[0], ya[1], yb[0], yb[1]}, rw, yoff, 0, SC1);
  if (hw != 0) return;
  {
    u32x4 e0[4], e1[4];
    float s0 = 0.f, s1 = 0.f;
    auto fetch = [&](u32x4 (&d)[4]) {
#pragma unroll
      for (int k = 0; k < 4; ++k) d[k] = __builtin_amdgcn_raw_buffer_load_b128(rw, gath, k * GSTR, SC1);
      PIN();
    };
    auto there = [&](const u32x4& d) { return pair_valid(d, ytag0, ytag1); };
    auto take = [&](const u32x4 (&d)[4]) {
      float a0 = 0.f, a1 = 0.f, b0 = 0.f, b1 = 0.f;
#pragma unroll
      for (int k = 0; k < 4; ++k) { a0 += pair_first(d[k][1]); a1 += pair_second(d[k][0], d[k][1]); b0 += pair_first(d[k][3]); b1 += pair_second(d[k][2], d[k][3]); }
      a0 += dpp<XOR2>(a0); a1 += dpp<XOR2>(a1); b0 += dpp<XOR2>(b0); b1 += dpp<XOR2>(b1);
      s0 = (lane & 2) ? b0 : a0;
      s1 = (lane & 2) ? b1 : a1;
      return (bool)__all((int)there(d[0]) & (int)there(d[1]) & (int)there(d[2]) & (int)there(d[3]));
    };
    __builtin_amdgcn_s_sleep(POLL_T0);
    fetch(e0); __builtin_amdgcn_s_sleep(POLL_GAP);
    fetch(e1);
    bool all = take(e0);
    if (!all) {
      const unsigned ts = now();
      while (!(all = take(e1)) && now() - ts <= SPIN_LIMIT) {
        u32x4 n[4];
#pragma unroll
        for (int k = 0; k < 4; ++k) n[k] = __builtin_amdgcn_raw_buffer_load_b128(rw, there(e1[k]) ? WS_END : gath + k * GSTR, 0, SC1);
#pragma unroll
        for (int k = 0; k < 4; ++k) if (!there(e1[k])) e1[k] = n[k];
      }
    }
    s0 += dpp<XOR8>(s0); s1 += dpp<XOR8>(s1);
    s0 += dpp<ROR4>(s0); s1 += dpp<ROR4>(s1);
    s0 = swap16_add(s0, s0); s1 = swap16_add(s1, s1);
    s0 = swap32_add(s0, s0); s1 = swap32_add(s1, s1);
    if (!(ok && all)) { s0 = __builtin_nanf(""); s1 = s0; }
    const auto rout = __builtin_amdgcn_make_buffer_rsrc((void*)pout, 0, 2 * 2 * DM * 2, 0x00020000);
    if (lane < 4) __builtin_amdgcn_raw_buffer_store_b32(pack_bf16(s0, s1), rout, (b * 2 * DM + (lane >> 1) * DM + mem * 4) * 2 + (lane & 1) * 4, 0, 0);
    if (mem == 0 && lane == 0 && all)
      __hip_atomic_store((GAS unsigned long long*)(pws + WS_EG + b * LINE), ((unsigned long long)thi << 32) | tlo, __ATOMIC_RELAXED, __HIP_MEMORY_SCOPE_AGENT);
    PIN();
    asm volatile("" :: "v"(e1[0]), "v"(e1[1]), "v"(e1[2]), "v"(e1[3]));
  }
}

extern "C" void kernel_launch(void* const* d_in, const int* in_sizes, int n_in, void* d_out, int out_size, void* d_ws,
                              size_t ws_size, hipStream_t stream) {
  Params P;
  for (int p = 0; p < 2; ++p) {
    P.x[p] = (const unsigned char*)d_in[5 * p + 0];
    P.k[p] = (const unsigned char*)d_in[5 * p + 1];
    P.v[p] = (const unsigned char*)d_in[5 * p + 2];
    P.wq[p] = (const unsigned char*)d_in[5 * p + 3];
    P.wo[p] = (const unsigned char*)d_in[5 * p + 4];
  }
  P.out = (unsigned char*)d_out;
  P.ws = (unsigned char*)d_ws + (ws_size > (size_t)(8u << 20) ? ((ws_size - WS_END - (1u << 20)) & ~(size_t)0x1fffff) : 0);
  mha_decode_kernel<<<NWG, NTHR, 0, stream>>>(P);
}
```
